```python
import torch

def _softmax(x, axis=-1):
    return torch.softmax(x, dim=axis)
def _gelu(x):
    return torch.nn.functional.gelu(x, approximate="tanh")
def _silu(x):
    return torch.nn.functional.silu(x)
def _sigmoid(x):
    return torch.sigmoid(x)
def _layernorm(x, axis=-1, eps=1e-5):
    m = x.mean(dim=axis, keepdim=True)
    v = x.var(dim=axis, keepdim=True, unbiased=False)
    return (x - m) / torch.sqrt(v + eps)
def _scatter_add(tgt, idx, src, axis=0):
    return tgt.clone().index_add_(axis, idx, src)
def _segment_sum(src, seg, n):
    out = torch.zeros((n,) + src.shape[1:], dtype=src.dtype)
    return out.index_add_(0, seg, src)
def _pad(x, pw):
    flat = tuple(v for pair in reversed(pw) for v in pair)
    return torch.nn.functional.pad(x, flat)

kernel_name = 'standalone__mha_block_cached__near_ridge'

def setup_inputs():

    _w = (lambda a: (lambda t: t.to(torch.bfloat16) if t.is_floating_point() else t)(torch.as_tensor(a)))
    B, Sq, D = 8, 128, 512
    H, Sk, Dh = 32, 2048, 128
    return {
        "x": _w((lambda *s: torch.randn(*s, dtype=torch.bfloat16) * 0.8)(B, Sq, D)),
        "k_cache": _w((lambda *s: torch.randn(*s, dtype=torch.bfloat16) * 0.8)(B, H, Sk, Dh)),
        "v_cache": _w((lambda *s: torch.randn(*s, dtype=torch.bfloat16) * 0.05)(B, H, Sk, Dh)),
        "wq": _w((lambda *s: torch.randn(*s, dtype=torch.bfloat16) * 0.8)(D, H * Dh) * (D ** -0.5)),
        "wo": _w((lambda *s: torch.randn(*s, dtype=torch.bfloat16) * 0.05)(H * Dh, D) * ((H * Dh) ** -0.5)),
    }

def reference(x, k_cache, v_cache, wq, wo):
    B, Sq, D = x.shape
    H, Dh = 32, 128
    q = (lambda x, *d: x.permute(*d))((x @ wq).reshape(B, Sq, H, Dh), 0, 2, 1, 3)
    scores = torch.einsum("bhqd,bhkd->bhqk", q, k_cache) * (Dh ** -0.5)
    p = _softmax(scores, axis=-1)
    o = torch.einsum("bhqk,bhkd->bhqd", p, v_cache)
    return (lambda x, *d: x.permute(*d))(o, 0, 2, 1, 3).reshape(B, Sq, H * Dh) @ wo
```

```cpp
#include <hip/hip_runtime.h>
#include <hip/hip_bf16.h>

typedef __bf16 bf16x8 __attribute__((ext_vector_type(8)));
typedef __bf16 bf16x4 __attribute__((ext_vector_type(4)));
typedef __bf16 bf16x2 __attribute__((ext_vector_type(2)));
typedef float f32x16 __attribute__((ext_vector_type(16)));
typedef float f32x4 __attribute__((ext_vector_type(4)));
typedef float f32x2 __attribute__((ext_vector_type(2)));
typedef unsigned u32x4 __attribute__((ext_vector_type(4)));
#define LAS __attribute__((address_space(3)))
#define DI __device__ __forceinline__
#define SBAR() __builtin_amdgcn_sched_barrier(0)
#define PIN(x) asm volatile("" : "+v"(x))
#define MFMA(a, b, c) __builtin_amdgcn_mfma_f32_32x32x16_bf16((a), (b), (c), 0, 0, 0)

constexpr int NB = 8, SQ = 128, DM = 512, NH = 32, SK = 2048, DH = 128;
constexpr unsigned PS = 32768u;
constexpr unsigned VOFF = 16384u;
constexpr float QSCALE = 0.08838834764831845f * 1.4426950408889634f;
constexpr float SUM_THR = 65536.0f;

DI unsigned rfl(unsigned x) { return __builtin_amdgcn_readfirstlane(x); }
DI unsigned cvtpk(float lo, float hi) { f32x2 v = {lo, hi}; bf16x2 b = __builtin_convertvector(v, bf16x2); return __builtin_bit_cast(unsigned, b); }
DI bf16x8 lds_rd128(unsigned addr) { return *(const LAS bf16x8*)(size_t)addr; }
DI bf16x4 lds_rdtr(unsigned addr) { return __builtin_amdgcn_ds_read_tr16_b64_v4bf16((LAS bf16x4*)(size_t)addr); }
DI bf16x8 cat8(bf16x4 a, bf16x4 b) { return __builtin_shufflevector(a, b, 0, 1, 2, 3, 4, 5, 6, 7); }
DI void lds_wr128(unsigned addr, u32x4 v) { *(LAS u32x4*)(size_t)addr = v; }
DI float swap32_max(float v) { auto r = __builtin_amdgcn_permlane32_swap(__float_as_uint(v), __float_as_uint(v), false, false); return __builtin_fmaxf(__uint_as_float(r[0]), __uint_as_float(r[1])); }
DI float swap32_sum(float v) { auto r = __builtin_amdgcn_permlane32_swap(__float_as_uint(v), __float_as_uint(v), false, false); return __uint_as_float(r[0]) + __uint_as_float(r[1]); }

#define DMA4(srd, voffA, voffB, soff, m0a) \
  asm volatile("s_mov_b32 m0, %0\n\ts_nop 0\n\t" \
               "buffer_load_dwordx4 %2, %4, %5 offen nt lds\n\t" \
               "buffer_load_dwordx4 %3, %4, %5 offen offset:2048 nt lds\n\t" \
               "s_mov_b32 m0, %1\n\ts_nop 0\n\t" \
               "buffer_load_dwordx4 %2, %4, %5 offen offset:128 nt lds\n\t" \
               "buffer_load_dwordx4 %3, %4, %5 offen offset:2176 nt lds" \
               :: "s"(m0a), "s"((m0a) + 896u), "v"(voffA), "v"(voffB), "s"(srd), "s"(soff) : "memory")

#define BAR_ONLY() asm volatile("s_waitcnt lgkmcnt(0)\n\ts_barrier" ::: "memory")
#define WAIT_BAR(N) asm volatile("s_waitcnt vmcnt(" #N ") lgkmcnt(0)\n\ts_barrier" ::: "memory")

__global__ __launch_bounds__(512, 2) void attn_kernel(const char* __restrict__ x, const char* __restrict__ kc, const char* __restrict__ vc,
                                                      const char* __restrict__ wq, char* __restrict__ ofrag) {
  __shared__ __attribute__((aligned(1024))) char lds[163840];
  const unsigned tid = threadIdx.x, lane = tid & 63u, l32 = lane & 31u, hi = lane >> 5;
  const unsigned w = rfl(tid >> 6), wq4 = w & 3u, g = w >> 2;
  const unsigned blk = blockIdx.x, xcd = blk & 7u, j = blk >> 3;
  const unsigned b = 4u * (xcd & 1u) + (j >> 3), h = 8u * (xcd >> 1) + (j & 7u), bh = b * NH + h;
  const unsigned L0 = (unsigned)(size_t)lds;

  const __amdgpu_buffer_rsrc_t srdX = __builtin_amdgcn_make_buffer_rsrc((void*)(x + (size_t)b * (SQ * DM * 2)), (short)0, SQ * DM * 2, 0x00020000);
  const __amdgpu_buffer_rsrc_t srdW = __builtin_amdgcn_make_buffer_rsrc((void*)(wq + (size_t)h * 256), (short)0, (int)(DM * 8192u - h * 256u), 0x00020000);
  const __amdgpu_buffer_rsrc_t srdK = __builtin_amdgcn_make_buffer_rsrc((void*)(kc + (size_t)bh * (SK * 256)), (short)0, SK * 256, 0x00020000);
  const __amdgpu_buffer_rsrc_t srdV = __builtin_amdgcn_make_buffer_rsrc((void*)(vc + (size_t)bh * (SK * 256)), (short)0, SK * 256, 0x00020000);

  const unsigned sub = lane >> 5, r8 = (lane >> 2) & 7u, slot = lane & 3u;
  u32x4 xr[8][2], wr[8][2];
  {
    const unsigned vx0 = (16u * w + r8) * 1024u + 64u * sub + 16u * (slot ^ (r8 >> 2));
    const unsigned vx1 = (16u * w + 8u + r8) * 1024u + 64u * sub + 16u * (slot ^ (2u + (r8 >> 2)));
    const unsigned vw = (8u * w + r8) * 8192u + 64u * sub + 16u * slot;
#pragma unroll
    for (int c = 0; c < 8; ++c) {
      asm volatile("buffer_load_dwordx4 %0, %1, %2, 0 offen offset:%3" : "=v"(xr[c][0]) : "v"(vx0), "s"(srdX), "i"(128 * c));
      asm volatile("buffer_load_dwordx4 %0, %1, %2, 0 offen offset:%3" : "=v"(xr[c][1]) : "v"(vx1), "s"(srdX), "i"(128 * c));
      asm volatile("buffer_load_dwordx4 %0, %1, %2, %3 offen" : "=v"(wr[c][0]) : "v"(vw), "s"(srdW), "s"(524288u * c));
      asm volatile("buffer_load_dwordx4 %0, %1, %2, %3 offen offset:128" : "=v"(wr[c][1]) : "v"(vw), "s"(srdW), "s"(524288u * c));
    }
  }
  const unsigned vkA = r8 * 256u + 64u * sub + 16u * (slot ^ (r8 >> 2));
  const unsigned vkB = r8 * 256u + 64u * sub + 16u * (slot ^ (2u + (r8 >> 2)));
  const unsigned vvA = r8 * 256u + 64u * sub + 16u * slot;
  const unsigned wrow = wq4 * 4096u;
  {
    const unsigned p0 = g ? 0u : 1u, p1 = p0 + 2u;
    DMA4(srdK, vkA, vkB, p0 * 16384u + wrow, L0 + p0 * PS + wrow);
    DMA4(srdK, vkA, vkB, p1 * 16384u + wrow, L0 + p1 * PS + wrow);
    DMA4(srdV, vvA, vvA, (p1 - 2u) * 16384u + wrow, L0 + p1 * PS + VOFF + wrow);
  }
  bf16x8 qf[8];
  {
    f32x16 qa0 = {}, qa1 = {};
    const unsigned xrd0 = 4096u * wq4 + 1024u * (l32 >> 3) + 64u * (l32 & 7u) + 16u * ((0u + hi) ^ ((l32 >> 2) & 3u));
    const unsigned xrd1 = 4096u * wq4 + 1024u * (l32 >> 3) + 64u * (l32 & 7u) + 16u * ((2u + hi) ^ ((l32 >> 2) & 3u));
    const unsigned p = lane & 3u, psw = ((p & 1u) << 1) | (p >> 1);
    const unsigned wrd = 2048u * hi + 64u * ((lane >> 2) & 3u) + 32u * ((lane >> 4) & 1u) + 8u * psw + 1024u * g;
    const unsigned stw = 2048u * w + 16u * lane;
#define XB(c) (L0 + (((c) & 1) ? 4u * PS : VOFF))
#define WB(c) (L0 + (((c) & 1) ? 4u * PS + VOFF : PS + VOFF))
#define QSTAGE(c, N) do { asm volatile("s_waitcnt vmcnt(" #N ")" : "+v"(xr[c][0]), "+v"(xr[c][1]), "+v"(wr[c][0]), "+v"(wr[c][1])); \
      lds_wr128(XB(c) + stw, xr[c][0]); lds_wr128(XB(c) + stw + 1024u, xr[c][1]); lds_wr128(WB(c) + stw, wr[c][0]); lds_wr128(WB(c) + stw + 1024u, wr[c][1]); } while (0)
#define QCOMP(c) do { _Pragma("unroll") for (int s = 0; s < 4; ++s) { \
      const bf16x8 bx = lds_rd128(XB(c) + ((s & 1) ? xrd1 : xrd0) + 512u * (s >> 1)); \
      const bf16x8 a0 = cat8(lds_rdtr(WB(c) + wrd + 4096u * s), lds_rdtr(WB(c) + wrd + 4096u * s + 256u)); \
      const bf16x8 a1 = cat8(lds_rdtr(WB(c) + wrd + 4096u * s + 512u), lds_rdtr(WB(c) + wrd + 4096u * s + 768u)); \
      qa0 = MFMA(a0, bx, qa0); qa1 = MFMA(a1, bx, qa1); } } while (0)
    QSTAGE(0, 40); BAR_ONLY();
    QSTAGE(1, 36); QCOMP(0); BAR_ONLY();
    QSTAGE(2, 32); QCOMP(1); BAR_ONLY();
    QSTAGE(3, 28); QCOMP(2); BAR_ONLY();
    QSTAGE(4, 24); QCOMP(3); BAR_ONLY();
    QSTAGE(5, 20); QCOMP(4); BAR_ONLY();
    QSTAGE(6, 16); QCOMP(5); BAR_ONLY();
    QSTAGE(7, 12); QCOMP(6); BAR_ONLY();
    QCOMP(7);
    u32x4 mine[4];
#pragma unroll
    for (int sp = 0; sp < 2; ++sp) {
#pragma unroll
      for (int e = 0; e < 4; ++e) {
        mine[sp][e] = cvtpk(qa0[8 * sp + 2 * e] * QSCALE, qa0[8 * sp + 2 * e + 1] * QSCALE);
        mine[2 + sp][e] = cvtpk(qa1[8 * sp + 2 * e] * QSCALE, qa1[8 * sp + 2 * e + 1] * QSCALE);
      }
    }
    const unsigned qx_w = L0 + VOFF + g * PS + 4096u * wq4 + 16u * lane, qx_r = L0 + VOFF + (g ^ 1u) * PS + 4096u * wq4 + 16u * lane;
#pragma unroll
    for (int i = 0; i < 4; ++i) lds_wr128(qx_w + 1024u * i, mine[i]);
    WAIT_BAR(8);
#pragma unroll
    for (int i = 0; i < 4; ++i) {
      const bf16x8 oth = lds_rd128(qx_r + 1024u * i);
      const bf16x8 own = __builtin_bit_cast(bf16x8, mine[i]);
      qf[i] = g ? oth : own; qf[4 + i] = g ? own : oth;
    }
#undef QSTAGE
#undef QCOMP
  }

  const unsigned krd0 = L0 + 2048u * (l32 >> 3) + 64u * (l32 & 7u) + 16u * ((0u + hi) ^ ((l32 >> 2) & 3u));
  const unsigned krd1 = L0 + 2048u * (l32 >> 3) + 64u * (l32 & 7u) + 16u * ((2u + hi) ^ ((l32 >> 2) & 3u));
  const unsigned vrd = L0 + VOFF + 8u * (lane & 3u) + 64u * ((lane >> 2) & 3u) + 32u * ((lane >> 4) & 1u) + 256u * hi;
  f32x16 o0 = {}, o1 = {}, o2 = {}, o3 = {}, s0, s1, negm = {};
  PIN(negm);
  float mref = 0.f, lsum = 0.f;
  u32x4 pf[4] = {};
  bf16x8 fr[8];
#define KFR(m, sl) lds_rd128((((m) >> 1) & 1 ? krd1 : krd0) + (sl) + 8192u * ((m) & 1) + 512u * ((m) >> 2))
#define VFR(m, sl) cat8(lds_rdtr(vrd + (sl) + 4096u * ((m) >> 2) + 512u * ((m) & 3)), lds_rdtr(vrd + (sl) + 4096u * ((m) >> 2) + 512u * ((m) & 3) + 2048u))
  constexpr int LA = 6;
  unsigned sl = g * PS;
  if (g) {
    DMA4(srdK, vkA, vkB, 4u * 16384u + wrow, L0 + 4u * PS + wrow);
    DMA4(srdV, vvA, vvA, 2u * 16384u + wrow, L0 + 4u * PS + VOFF + wrow);
  }
#pragma unroll
  for (int m = 0; m < LA; ++m) fr[m] = KFR(m, sl);
  if (g) { WAIT_BAR(8); }
  for (unsigned t = 0; t < 16; ++t) {
    const unsigned i = 2u * t + g;
    __builtin_amdgcn_s_setprio(1);
    SBAR();
#pragma unroll
    for (int m = 0; m < 16; ++m) {
      if (m + LA < 16) fr[(m + LA) & 7] = KFR(m + LA, sl);
      else fr[(m + LA) & 7] = VFR(m + LA - 16, sl);
      if (m == 0) s0 = MFMA(fr[0], qf[0], negm);
      else if (m == 1) s1 = MFMA(fr[1], qf[0], negm);
      else if (m & 1) s1 = MFMA(fr[m & 7], qf[m >> 1], s1);
      else s0 = MFMA(fr[m & 7], qf[m >> 1], s0);
      SBAR();
    }
    if (t > 0) {
#pragma unroll
      for (int m = 0; m < 16; ++m) {
        if (m + LA < 16) fr[(m + LA) & 7] = VFR(m + LA, sl);
        const bf16x8 pb = __builtin_bit_cast(bf16x8, pf[m >> 2]);
        if ((m & 3) == 0) o0 = MFMA(fr[m & 7], pb, o0);
        else if ((m & 3) == 1) o1 = MFMA(fr[m & 7], pb, o1);
        else if ((m & 3) == 2) o2 = MFMA(fr[m & 7], pb, o2);
        else o3 = MFMA(fr[m & 7], pb, o3);
        SBAR();
      }
    }
    __builtin_amdgcn_s_setprio(0);
    BAR_ONLY();
    const unsigned pn = i + 5u;
    if (pn <= 31u) DMA4(srdK, vkA, vkB, pn * 16384u + wrow, L0 + sl + wrow);
    if (pn <= 33u) DMA4(srdV, vvA, vvA, (pn - 2u) * 16384u + wrow, L0 + sl + VOFF + wrow);
    sl += 2u * PS; if (sl >= 5u * PS) sl -= 5u * PS;
    float ts;
#define EXPS() do { float a0 = 0.f, a1 = 0.f; \
      _Pragma("unroll") for (int e = 0; e < 8; ++e) { const float e0 = __builtin_amdgcn_exp2f(s0[2 * e]), e1 = __builtin_amdgcn_exp2f(s0[2 * e + 1]); a0 += e0; a1 += e1; pf[e >> 2][e & 3] = cvtpk(e0, e1); } \
      _Pragma("unroll") for (int e = 0; e < 8; ++e) { const float e0 = __builtin_amdgcn_exp2f(s1[2 * e]), e1 = __builtin_amdgcn_exp2f(s1[2 * e + 1]); a0 += e0; a1 += e1; pf[2 + (e >> 2)][e & 3] = cvtpk(e0, e1); } \
      ts = a0 + a1; } while (0)
    bool redo = (t == 0);
    if (!redo) { EXPS(); redo = __any(!(ts <= SUM_THR)); }
    if (__builtin_expect(redo, 0)) {
      float mx = s0[0];
#pragma unroll
      for (int e = 1; e < 16; ++e) mx = __builtin_fmaxf(mx, s0[e]);
#pragma unroll
      for (int e = 0; e < 16; ++e) mx = __builtin_fmaxf(mx, s1[e]);
      mx = swap32_max(mx);
      const float dl = (t == 0) ? mx : __builtin_fmaxf(mx, 0.f);
      const float f = (t == 0) ? 1.f : __builtin_amdgcn_exp2f(-dl);
      mref += dl; lsum *= f;
#pragma unroll
      for (int e = 0; e < 16; ++e) { s0[e] -= dl; s1[e] -= dl; o0[e] *= f; o1[e] *= f; o2[e] *= f; o3[e] *= f; negm[e] = -mref; }
      PIN(negm);
      EXPS();
    }
    lsum += ts;
    SBAR();
    if (t < 15) {
#pragma unroll
      for (int m = 0; m < LA; ++m) fr[m] = KFR(m, sl);
    } else {
#pragma unroll
      for (int m = 0; m < LA; ++m) fr[m] = VFR(m, sl);
    }
    if (pn <= 31u) { WAIT_BAR(8); } else if (pn <= 33u) { WAIT_BAR(4); } else { WAIT_BAR(0); }
  }
#pragma unroll
  for (int m = 0; m < 16; ++m) {
    if (m + LA < 16) fr[(m + LA) & 7] = VFR(m + LA, sl);
    const bf16x8 pb = __builtin_bit_cast(bf16x8, pf[m >> 2]);
    if ((m & 3) == 0) o0 = MFMA(fr[m & 7], pb, o0);
    else if ((m & 3) == 1) o1 = MFMA(fr[m & 7], pb, o1);
    else if ((m & 3) == 2) o2 = MFMA(fr[m & 7], pb, o2);
    else o3 = MFMA(fr[m & 7], pb, o3);
    SBAR();
  }
  if (!g) { BAR_ONLY(); }
  lsum = swap32_sum(lsum);
  {
    const unsigned ow = L0 + 8192u * w + 16u * lane, orr = L0 + 8192u * (w ^ 4u) + 16u * lane;
    const f32x16 sa = g ? o0 : o2, sb = g ? o1 : o3;
#pragma unroll
    for (int q = 0; q < 4; ++q) {
      *(LAS f32x4*)(size_t)(ow + 1024u * q) = (f32x4){sa[4 * q], sa[4 * q + 1], sa[4 * q + 2], sa[4 * q + 3]};
      *(LAS f32x4*)(size_t)(ow + 4096u + 1024u * q) = (f32x4){sb[4 * q], sb[4 * q + 1], sb[4 * q + 2], sb[4 * q + 3]};
    }
    *(LAS f32x2*)(size_t)(L0 + 2u * PS + 512u * w + 8u * lane) = (f32x2){mref, lsum};
    BAR_ONLY();
    const f32x2 st = *(const LAS f32x2*)(size_t)(L0 + 2u * PS + 512u * (w ^ 4u) + 8u * lane);
    const float mm = __builtin_fmaxf(mref, st[0]);
    const float fa = __builtin_amdgcn_exp2f(mref - mm), fb = __builtin_amdgcn_exp2f(st[0] - mm);
    const float inv = 1.0f / (fa * lsum + fb * st[1]);
    const float ca = fa * inv, cb = fb * inv;
    const f32x16 ka = g ? o2 : o0, kb = g ? o3 : o1;
    char* dst = ofrag + ((size_t)((b * 4u + wq4) * NH + h) * 8u + 4u * g) * 1024u + 16u * lane;
#pragma unroll
    for (int d = 0; d < 2; ++d) {
#pragma unroll
      for (int sp = 0; sp < 2; ++sp) {
        const f32x4 u0 = *(const LAS f32x4*)(size_t)(orr + 4096u * d + 1024u * (2 * sp)), u1 = *(const LAS f32x4*)(size_t)(orr + 4096u * d + 1024u * (2 * sp + 1));
        const f32x16& own = d ? kb : ka;
        u32x4 r;
        r[0] = cvtpk(own[8 * sp + 0] * ca + u0[0] * cb, own[8 * sp + 1] * ca + u0[1] * cb);
        r[1] = cvtpk(own[8 * sp + 2] * ca + u0[2] * cb, own[8 * sp + 3] * ca + u0[3] * cb);
        r[2] = cvtpk(own[8 * sp + 4] * ca + u1[0] * cb, own[8 * sp + 5] * ca + u1[1] * cb);
        r[3] = cvtpk(own[8 * sp + 6] * ca + u1[2] * cb, own[8 * sp + 7] * ca + u1[3] * cb);
        *(u32x4*)(dst + (2 * d + sp) * 1024) = r;
      }
    }
  }
}

__global__ __launch_bounds__(512, 2) void oproj_kernel(const char* __restrict__ ofrag, const char* __restrict__ wo, char* __restrict__ out) {
  __shared__ __attribute__((aligned(1024))) char lds[131072];
  const unsigned tid = threadIdx.x, lane = tid & 63u, l32 = lane & 31u, hi = lane >> 5;
  const unsigned w = rfl(tid >> 6);
  const unsigned blk = blockIdx.x, xcd = blk & 7u, j = blk >> 3;
  const unsigned b = 2u * (xcd >> 1) + (j >> 4), qb = (j >> 2) & 3u, ct = 4u * (xcd & 1u) + (j & 3u);
  const unsigned L0 = (unsigned)(size_t)lds + 16384u * w;
  const __amdgpu_buffer_rsrc_t srdW = __builtin_amdgcn_make_buffer_rsrc((void*)(wo + (size_t)w * 512u * 1024u + ct * 128u), (short)0, (int)(512u * 1024u - ct * 128u), 0x00020000);
  const unsigned vsrc = ((lane >> 2) & 7u) * 1024u + 64u * hi + 16u * (lane & 3u);
  const char* osrc = ofrag + ((size_t)((b * 4u + qb) * NH + 4u * w) * 8u) * 1024u + 16u * lane;
  u32x4 of[32];
#pragma unroll
  for (int s = 0; s < 32; ++s) asm volatile("global_load_dwordx4 %0, %1, off" : "=v"(of[s]) : "v"(osrc + s * 1024));
#define WDMA(pc) asm volatile("s_mov_b32 m0, %0\n\ts_nop 0\n\tbuffer_load_dwordx4 %1, %2, %3 offen lds" :: "s"(L0 + 1024u * ((pc) & 15)), "v"(vsrc), "s"(srdW), "s"(8192u * (pc)) : "memory")
#pragma unroll
  for (int pc = 0; pc < 16; ++pc) WDMA(pc);
  const unsigned ard = L0 + 8u * (lane & 3u) + 64u * ((lane >> 2) & 3u) + 32u * ((lane >> 4) & 1u) + 256u * hi;
  f32x16 c0 = {}, c1 = {};
#pragma unroll
  for (int s = 0; s < 32; s += 2) {
    if (s < 24) asm volatile("s_waitcnt vmcnt(12)" : "+v"(of[s]), "+v"(of[s + 1]) :: "memory");
    else if (s == 24) asm volatile("s_waitcnt vmcnt(12)" : "+v"(of[s]), "+v"(of[s + 1]) :: "memory");
    else if (s == 26) asm volatile("s_waitcnt vmcnt(8)" : "+v"(of[s]), "+v"(of[s + 1]) :: "memory");
    else if (s == 28) asm volatile("s_waitcnt vmcnt(4)" : "+v"(of[s]), "+v"(of[s + 1]) :: "memory");
    else asm volatile("s_waitcnt vmcnt(0)" : "+v"(of[s]), "+v"(of[s + 1]) :: "memory");
    bf16x8 a[2][2];
#pragma unroll
    for (int u = 0; u < 2; ++u) {
      const unsigned base = ard + 1024u * ((2 * (s + u)) & 15);
      a[u][0] = cat8(lds_rdtr(base), lds_rdtr(base + 1024u));
      a[u][1] = cat8(lds_rdtr(base + 512u), lds_rdtr(base + 1536u));
    }
    asm volatile("s_waitcnt lgkmcnt(0)" : "+v"(a[0][0]), "+v"(a[0][1]), "+v"(a[1][0]), "+v"(a[1][1]) :: "memory");
    if (2 * s + 16 < 64) { WDMA(2 * s + 16); WDMA(2 * s + 17); WDMA(2 * s + 18); WDMA(2 * s + 19); }
#pragma unroll
    for (int u = 0; u < 2; ++u) {
      c0 = MFMA(a[u][0], __builtin_bit_cast(bf16x8, of[s + u]), c0);
      c1 = MFMA(a[u][1], __builtin_bit_cast(bf16x8, of[s + u]), c1);
    }
  }
  __syncthreads();
  float* red = (float*)lds;
#pragma unroll
  for (int q = 0; q < 4; ++q) {
    *(f32x4*)(red + (w * 32u + l32) * 68u + 8u * q + 4u * hi) = (f32x4){c0[4 * q], c0[4 * q + 1], c0[4 * q + 2], c0[4 * q + 3]};
    *(f32x4*)(red + (w * 32u + l32) * 68u + 32u + 8u * q + 4u * hi) = (f32x4){c1[4 * q], c1[4 * q + 1], c1[4 * q + 2], c1[4 * q + 3]};
  }
  __syncthreads();
  {
    const unsigned q = tid >> 4, c4 = (tid & 15u) * 4u;
    f32x4 acc = *(const f32x4*)(red + q * 68u + c4);
#pragma unroll
    for (int ww = 1; ww < 8; ++ww) acc += *(const f32x4*)(red + (ww * 32u + q) * 68u + c4);
    uint2 r; r.x = cvtpk(acc[0], acc[1]); r.y = cvtpk(acc[2], acc[3]);
    *(uint2*)(out + ((size_t)(b * SQ + qb * 32u + q) * DM + ct * 64u + c4) * 2u) = r;
  }
}

extern "C" void kernel_launch(void* const* d_in, const int* in_sizes, int n_in, void* d_out, int out_size, void* d_ws, size_t ws_size, hipStream_t stream) {
  const char* x = (const char*)d_in[0];
  const char* kc = (const char*)d_in[1];
  const char* vc = (const char*)d_in[2];
  const char* wq = (const char*)d_in[3];
  const char* wo = (const char*)d_in[4];
  char* ofrag = (char*)d_ws;
  attn_kernel<<<256, 512, 0, stream>>>(x, kc, vc, wq, ofrag);
  oproj_kernel<<<256, 512, 0, stream>>>(ofrag, wo, (char*)d_out);
}
```

```cpp
#include <hip/hip_runtime.h>
#include <hip/hip_bf16.h>

typedef __bf16 bf16x8 __attribute__((ext_vector_type(8)));
typedef __bf16 bf16x4 __attribute__((ext_vector_type(4)));
typedef __bf16 bf16x2 __attribute__((ext_vector_type(2)));
typedef float f32x16 __attribute__((ext_vector_type(16)));
typedef float f32x4 __attribute__((ext_vector_type(4)));
typedef float f32x2 __attribute__((ext_vector_type(2)));
typedef unsigned u32x4 __attribute__((ext_vector_type(4)));
typedef unsigned long long u64;
#define LAS __attribute__((address_space(3)))
#define GAS __attribute__((address_space(1)))
#define DI __device__ __forceinline__
#define SBAR() __builtin_amdgcn_sched_barrier(0)
#define PIN(x) asm volatile("" : "+v"(x))
#define MFMA(a, b, c) __builtin_amdgcn_mfma_f32_32x32x16_bf16((a), (b), (c), 0, 0, 0)

#define XW_POL ""
#define WO_POL ""
#define WS_BASE 0u
constexpr int NB = 8, SQ = 128, DM = 512, NH = 32, SK = 2048, DH = 128;
constexpr unsigned PS = 32768u;
constexpr unsigned VOFF = 16384u;
constexpr float QSCALE = 0.08838834764831845f * 1.4426950408889634f;
constexpr float SUM_THR = 65536.0f;
constexpr unsigned WS_EPOCH = 0u, WS_FLAG1 = 4096u, WS_FLAG2 = 16384u, WS_OFRAG = 1u << 20, WS_PART = 16u << 20;
constexpr unsigned SPIN_CAP = 1u << 18;

DI unsigned rfl(unsigned x) { return __builtin_amdgcn_readfirstlane(x); }
DI unsigned cvtpk(float lo, float hi) { f32x2 v = {lo, hi}; bf16x2 b = __builtin_convertvector(v, bf16x2); return __builtin_bit_cast(unsigned, b); }
DI bf16x8 lds_rd128(unsigned addr) { return *(const LAS bf16x8*)(size_t)addr; }
DI f32x4 lds_rdf4(unsigned addr) { return *(const LAS f32x4*)(size_t)addr; }
DI bf16x4 lds_rdtr(unsigned addr) { return __builtin_amdgcn_ds_read_tr16_b64_v4bf16((LAS bf16x4*)(size_t)addr); }
DI bf16x8 cat8(bf16x4 a, bf16x4 b) { return __builtin_shufflevector(a, b, 0, 1, 2, 3, 4, 5, 6, 7); }
DI void lds_wr128(unsigned addr, u32x4 v) { *(LAS u32x4*)(size_t)addr = v; }
DI void lds_wrf4(unsigned addr, f32x4 v) { *(LAS f32x4*)(size_t)addr = v; }
DI float swap32_max(float v) { auto r = __builtin_amdgcn_permlane32_swap(__float_as_uint(v), __float_as_uint(v), false, false); return __builtin_fmaxf(__uint_as_float(r[0]), __uint_as_float(r[1])); }
DI float swap32_sum(float v) { auto r = __builtin_amdgcn_permlane32_swap(__float_as_uint(v), __float_as_uint(v), false, false); return __uint_as_float(r[0]) + __uint_as_float(r[1]); }
DI f32x4 quad(const f32x16& v, int q) { return (f32x4){v[4 * q], v[4 * q + 1], v[4 * q + 2], v[4 * q + 3]}; }
DI void poll_flags(const GAS u64* p, u64 tag, bool skip) {
  for (unsigned spin = 0; spin < SPIN_CAP; ++spin) {
    const u64 v = __hip_atomic_load(p, __ATOMIC_RELAXED, __HIP_MEMORY_SCOPE_AGENT);
    if (__all(skip || v == tag)) break;
    __builtin_amdgcn_s_sleep(2);
  }
  asm volatile("" ::: "memory");
}

#define DMA4(srd, voffA, voffB, soff, m0a) \
  asm volatile("s_mov_b32 m0, %0\n\ts_nop 0\n\t" \
               "buffer_load_dwordx4 %2, %4, %5 offen nt lds\n\t" \
               "buffer_load_dwordx4 %3, %4, %5 offen offset:2048 nt lds\n\t" \
               "s_mov_b32 m0, %1\n\ts_nop 0\n\t" \
               "buffer_load_dwordx4 %2, %4, %5 offen offset:128 nt lds\n\t" \
               "buffer_load_dwordx4 %3, %4, %5 offen offset:2176 nt lds" \
               :: "s"(m0a), "s"((m0a) + 896u), "v"(voffA), "v"(voffB), "s"(srd), "s"(soff) : "memory")
#define DMA1(pol, srd, voff, soff, m0a) \
  asm volatile("s_mov_b32 m0, %0\n\ts_nop 0\n\tbuffer_load_dwordx4 %1, %2, %3 offen" pol " lds" :: "s"(m0a), "v"(voff), "s"(srd), "s"(soff) : "memory")

#define STAMP(i)
#define BAR_ONLY() asm volatile("s_waitcnt lgkmcnt(0)\n\ts_barrier" ::: "memory")
#define WAIT_BAR(N) asm volatile("s_waitcnt vmcnt(" #N ") lgkmcnt(0)\n\ts_barrier" ::: "memory")

__global__ __launch_bounds__(512, 2) void mha_kernel(const char* __restrict__ x, const char* __restrict__ kc, const char* __restrict__ vc,
                                                     const char* __restrict__ wq, const char* __restrict__ wo, char* __restrict__ out, char* ws) {
  __shared__ __attribute__((aligned(1024))) char lds[163840];
  const unsigned tid = threadIdx.x, lane = tid & 63u, l32 = lane & 31u, hi = lane >> 5;
  const unsigned w = rfl(tid >> 6), wq4 = w & 3u, g = w >> 2;
  const unsigned blk = blockIdx.x, xcd = blk & 7u, j = blk >> 3;
  const unsigned b = 4u * (xcd & 1u) + (j >> 3), hg = xcd >> 1, im = j & 7u, h = 8u * hg + im, bh = b * NH + h;
  const unsigned L0 = (unsigned)(size_t)lds;
  STAMP(0);

  const __amdgpu_buffer_rsrc_t srdX = __builtin_amdgcn_make_buffer_rsrc((void*)(x + (size_t)b * (SQ * DM * 2)), (short)0, SQ * DM * 2, 0x00020000);
  const __amdgpu_buffer_rsrc_t srdW = __builtin_amdgcn_make_buffer_rsrc((void*)(wq + (size_t)h * 256), (short)0, (int)(DM * 8192u - h * 256u), 0x00020000);
  const __amdgpu_buffer_rsrc_t srdK = __builtin_amdgcn_make_buffer_rsrc((void*)(kc + (size_t)bh * (SK * 256)), (short)0, SK * 256, 0x00020000);
  const __amdgpu_buffer_rsrc_t srdV = __builtin_amdgcn_make_buffer_rsrc((void*)(vc + (size_t)bh * (SK * 256)), (short)0, SK * 256, 0x00020000);

  const unsigned sub = lane >> 5, r8 = (lane >> 2) & 7u, slot = lane & 3u;
  const unsigned vx0 = (16u * w + r8) * 1024u + 64u * sub + 16u * (slot ^ (r8 >> 2));
  const unsigned vx1 = (16u * w + 8u + r8) * 1024u + 64u * sub + 16u * (slot ^ (2u + (r8 >> 2)));
  const unsigned vw = (8u * w + r8) * 8192u + 64u * sub + 16u * slot;
  const unsigned crot = h + 2u * b;
#define XB(c) (L0 + ((c) % 5 == 0 ? 3u * PS : (c) % 5 == 1 ? VOFF : (c) % 5 == 2 ? 4u * PS : (c) % 5 == 3 ? 0u : 2u * PS))
#define WB(c) (L0 + ((c) % 5 == 0 ? 3u * PS + VOFF : (c) % 5 == 1 ? PS + VOFF : (c) % 5 == 2 ? 4u * PS + VOFF : (c) % 5 == 3 ? PS : 2u * PS + VOFF))
#define QL(c, k) do { const unsigned cc_ = ((c) + crot) & 7u; \
    if ((k) == 0) DMA1(XW_POL, srdX, vx0, 128u * cc_, XB(c) + 2048u * w); \
    else if ((k) == 1) DMA1(XW_POL, srdX, vx1, 128u * cc_, XB(c) + 2048u * w + 1024u); \
    else if ((k) == 2) DMA1(XW_POL, srdW, vw, 524288u * cc_, WB(c) + 2048u * w); \
    else DMA1(XW_POL, srdW, vw, 524288u * cc_ + 128u, WB(c) + 2048u * w + 1024u); } while (0)
#define QLOAD(c) do { QL(c, 0); QL(c, 1); QL(c, 2); QL(c, 3); } while (0)
  QLOAD(0);
  asm volatile("s_barrier" ::: "memory");
  QLOAD(1);
  u64 epoch;
  asm volatile("s_load_dwordx2 %0, %1, 0x0\n\ts_waitcnt lgkmcnt(0)" : "=s"(epoch) : "s"(ws + WS_EPOCH + 256u * b) : "memory");
  const u64 tag = epoch * 0x9E3779B97F4A7C15ull + 0xD1B54A32D192ED03ull;
  const unsigned vkA = r8 * 256u + 64u * sub + 16u * (slot ^ (r8 >> 2));
  const unsigned vkB = r8 * 256u + 64u * sub + 16u * (slot ^ (2u + (r8 >> 2)));
  const unsigned vvA = r8 * 256u + 64u * sub + 16u * slot;
  const unsigned wrow = wq4 * 4096u;
#define DA() DMA4(srdK, vkA, vkB, (g ^ 1u) * 16384u + wrow, L0 + (g ^ 1u) * PS + wrow)
#define DB() do { if (g) DMA4(srdK, vkA, vkB, 2u * 16384u + wrow, L0 + 2u * PS + wrow); else DMA4(srdV, vvA, vvA, wrow, L0 + 2u * PS + VOFF + wrow); } while (0)
#define DC() do { if (g) DMA4(srdV, vvA, vvA, 16384u + wrow, L0 + 3u * PS + VOFF + wrow); else DMA4(srdK, vkA, vkB, 3u * 16384u + wrow, L0 + 3u * PS + wrow); } while (0)
  STAMP(1);
  bf16x8 qf[8];
  {
    f32x16 qa0 = {}, qa1 = {};
    const unsigned xrd0 = 4096u * wq4 + 1024u * (l32 >> 3) + 64u * (l32 & 7u) + 16u * ((0u + hi) ^ ((l32 >> 2) & 3u));
    const unsigned xrd1 = 4096u * wq4 + 1024u * (l32 >> 3) + 64u * (l32 & 7u) + 16u * ((2u + hi) ^ ((l32 >> 2) & 3u));
    const unsigned p = lane & 3u, psw = ((p & 1u) << 1) | (p >> 1);
    const unsigned wrd = 2048u * hi + 64u * ((lane >> 2) & 3u) + 32u * ((lane >> 4) & 1u) + 8u * psw + 1024u * g;
    bf16x8 fx[2][4], fa[2][4], fb[2][4];
#define QREAD1(c, s) do { \
      fx[(c) & 1][s] = lds_rd128(XB(c) + ((s & 1) ? xrd1 : xrd0) + 512u * (s >> 1)); \
      fa[(c) & 1][s] = cat8(lds_rdtr(WB(c) + wrd + 4096u * s), lds_rdtr(WB(c) + wrd + 4096u * s + 256u)); \
      fb[(c) & 1][s] = cat8(lds_rdtr(WB(c) + wrd + 4096u * s + 512u), lds_rdtr(WB(c) + wrd + 4096u * s + 768u)); SBAR(); } while (0)
#define QMM(c, s) do { qa0 = MFMA(fa[(c) & 1][s], fx[(c) & 1][s], qa0); qa1 = MFMA(fb[(c) & 1][s], fx[(c) & 1][s], qa1); SBAR(); } while (0)
#define QSUB(c, s, LD) do { QMM((c) - 1, s); if (LD) { QL((c) + 3, s); SBAR(); } QREAD1(c, s); } while (0)
#define QSTEP(c, N, LD) do { WAIT_BAR(N); QSUB(c, 0, LD); QSUB(c, 1, LD); QSUB(c, 2, LD); QSUB(c, 3, LD); } while (0)
    WAIT_BAR(4); QREAD1(0, 0); QREAD1(0, 1); QREAD1(0, 2); QREAD1(0, 3); QLOAD(2); QLOAD(3); STAMP(2);
    QSTEP(1, 8, 1);
    QSTEP(2, 8, 1);
    QSTEP(3, 8, 1); STAMP(3);
    QSTEP(4, 8, 1); DA();
    QSTEP(5, 12, 0); DB();
    QSTEP(6, 12, 0); DC();
    QSTEP(7, 12, 0);
    QMM(7, 0); QMM(7, 1); QMM(7, 2); QMM(7, 3); STAMP(4);
    u32x4 mine[4];
#pragma unroll
    for (int sp = 0; sp < 2; ++sp) {
#pragma unroll
      for (int e = 0; e < 4; ++e) {
        mine[sp][e] = cvtpk(qa0[8 * sp + 2 * e] * QSCALE, qa0[8 * sp + 2 * e + 1] * QSCALE);
        mine[2 + sp][e] = cvtpk(qa1[8 * sp + 2 * e] * QSCALE, qa1[8 * sp + 2 * e + 1] * QSCALE);
      }
    }
    const unsigned qx_w = L0 + VOFF + g * PS + 4096u * wq4 + 16u * lane, qx_r = L0 + VOFF + (g ^ 1u) * PS + 4096u * wq4 + 16u * lane;
#pragma unroll
    for (int i = 0; i < 4; ++i) lds_wr128(qx_w + 1024u * i, mine[i]);
    WAIT_BAR(8);
#pragma unroll
    for (int i = 0; i < 4; ++i) {
      const bf16x8 oth = lds_rd128(qx_r + 1024u * i);
      const bf16x8 own = __builtin_bit_cast(bf16x8, mine[i]);
      qf[i] = g ? oth : own; qf[4 + i] = g ? own : oth;
    }
  }
  STAMP(5);
  const __amdgpu_buffer_rsrc_t srdO = __builtin_amdgcn_make_buffer_rsrc((void*)(wo + 128u * im), (short)0, (int)(4096u * 1024u - 128u * im), 0x00020000);
  const unsigned vwo = r8 * 1024u + 64u * sub + 16u * slot;
#define WDMA4(n, ldsa) do { const unsigned so_ = ((8u * hg + ((im + (n)) & 7u)) * 128u + 32u * wq4) * 1024u; \
    DMA1(WO_POL, srdO, vwo, so_, (ldsa) + wrow); DMA1(WO_POL, srdO, vwo, so_ + 8192u, (ldsa) + wrow + 1024u); \
    DMA1(WO_POL, srdO, vwo, so_ + 16384u, (ldsa) + wrow + 2048u); DMA1(WO_POL, srdO, vwo, so_ + 24576u, (ldsa) + wrow + 3072u); } while (0)

  const unsigned krd0 = L0 + 2048u * (l32 >> 3) + 64u * (l32 & 7u) + 16u * ((0u + hi) ^ ((l32 >> 2) & 3u));
  const unsigned krd1 = L0 + 2048u * (l32 >> 3) + 64u * (l32 & 7u) + 16u * ((2u + hi) ^ ((l32 >> 2) & 3u));
  const unsigned trl = 8u * (lane & 3u) + 64u * ((lane >> 2) & 3u) + 32u * ((lane >> 4) & 1u) + 256u * hi;
  const unsigned vrd = L0 + VOFF + trl;
  f32x16 o0 = {}, o1 = {}, o2 = {}, o3 = {}, s0, s1, negm = {};
  PIN(negm);
  float mref = 0.f, lsum = 0.f;
  u32x4 pf[4] = {};
  bf16x8 fr[8];
#define KFR(m, sl) lds_rd128((((m) >> 1) & 1 ? krd1 : krd0) + (sl) + 8192u * ((m) & 1) + 512u * ((m) >> 2))
#define VFR(m, sl) cat8(lds_rdtr(vrd + (sl) + 4096u * ((m) >> 2) + 512u * ((m) & 3)), lds_rdtr(vrd + (sl) + 4096u * ((m) >> 2) + 512u * ((m) & 3) + 2048u))
#define PVBLOCK() do { _Pragma("unroll") for (int m = 0; m < 16; ++m) { \
      if (m + LA < 16) fr[(m + LA) & 7] = VFR(m + LA, sl); \
      const bf16x8 pb = __builtin_bit_cast(bf16x8, pf[m >> 2]); \
      if ((m & 3) == 0) o0 = MFMA(fr[m & 7], pb, o0); \
      else if ((m & 3) == 1) o1 = MFMA(fr[m & 7], pb, o1); \
      else if ((m & 3) == 2) o2 = MFMA(fr[m & 7], pb, o2); \
      else o3 = MFMA(fr[m & 7], pb, o3); \
      SBAR(); } } while (0)
  constexpr int LA = 6;
  unsigned sl = g * PS;
#pragma unroll
  for (int m = 0; m < LA; ++m) fr[m] = KFR(m, sl);
  if (g) {
    DMA4(srdK, vkA, vkB, 4u * 16384u + wrow, L0 + 4u * PS + wrow);
    DMA4(srdV, vvA, vvA, 2u * 16384u + wrow, L0 + 4u * PS + VOFF + wrow);
    WAIT_BAR(12);
  }
  for (unsigned t = 0; t < 16; ++t) {
    const unsigned i = 2u * t + g;
    __builtin_amdgcn_s_setprio(1);
    SBAR();
#pragma unroll
    for (int m = 0; m < 16; ++m) {
      if (m + LA < 16) fr[(m + LA) & 7] = KFR(m + LA, sl);
      else fr[(m + LA) & 7] = VFR(m + LA - 16, sl);
      if (m == 0) s0 = MFMA(fr[0], qf[0], negm);
      else if (m == 1) s1 = MFMA(fr[1], qf[0], negm);
      else if (m & 1) s1 = MFMA(fr[m & 7], qf[m >> 1], s1);
      else s0 = MFMA(fr[m & 7], qf[m >> 1], s0);
      SBAR();
    }
    if (t > 0) PVBLOCK();
    __builtin_amdgcn_s_setprio(0);
    BAR_ONLY();
    const unsigned pn = i + 5u;
    if (pn <= 31u) DMA4(srdK, vkA, vkB, pn * 16384u + wrow, L0 + sl + wrow);
    else if (pn <= 35u) WDMA4(pn == 35u ? 4u : pn - 32u, L0 + sl);
    if (pn <= 33u) DMA4(srdV, vvA, vvA, (pn - 2u) * 16384u + wrow, L0 + sl + VOFF + wrow);
    else if (pn <= 35u) WDMA4(2u * pn - 65u, L0 + sl + VOFF);
    sl += 2u * PS; if (sl >= 5u * PS) sl -= 5u * PS;
    float ts;
#define EXPS() do { float a0 = 0.f, a1 = 0.f; \
      _Pragma("unroll") for (int e = 0; e < 8; ++e) { const float e0 = __builtin_amdgcn_exp2f(s0[2 * e]), e1 = __builtin_amdgcn_exp2f(s0[2 * e + 1]); a0 += e0; a1 += e1; pf[e >> 2][e & 3] = cvtpk(e0, e1); } \
      _Pragma("unroll") for (int e = 0; e < 8; ++e) { const float e0 = __builtin_amdgcn_exp2f(s1[2 * e]), e1 = __builtin_amdgcn_exp2f(s1[2 * e + 1]); a0 += e0; a1 += e1; pf[2 + (e >> 2)][e & 3] = cvtpk(e0, e1); } \
      ts = a0 + a1; } while (0)
    bool redo = (t == 0);
    if (!redo) { EXPS(); redo = __any(!(ts <= SUM_THR)); }
    if (__builtin_expect(redo, 0)) {
      float mx = s0[0];
#pragma unroll
      for (int e = 1; e < 16; ++e) mx = __builtin_fmaxf(mx, s0[e]);
#pragma unroll
      for (int e = 0; e < 16; ++e) mx = __builtin_fmaxf(mx, s1[e]);
      mx = swap32_max(mx);
      const float dl = (t == 0) ? mx : __builtin_fmaxf(mx, 0.f);
      const float f = (t == 0) ? 1.f : __builtin_amdgcn_exp2f(-dl);
      mref += dl; lsum *= f;
#pragma unroll
      for (int e = 0; e < 16; ++e) { s0[e] -= dl; s1[e] -= dl; o0[e] *= f; o1[e] *= f; o2[e] *= f; o3[e] *= f; negm[e] = -mref; }
      PIN(negm);
      EXPS();
    }
    lsum += ts;
    SBAR();
    if (t < 15) {
#pragma unroll
      for (int m = 0; m < LA; ++m) fr[m] = KFR(m, sl);
    } else {
#pragma unroll
      for (int m = 0; m < LA; ++m) fr[m] = VFR(m, sl);
    }
    WAIT_BAR(8);
  }
  STAMP(10);
  u32x4 own[4];
  {
    const unsigned xa = L0 + PS + 8192u * wq4 + 16u * lane;
    f32x4 oth[8];
    if (g) {
#pragma unroll
      for (int q = 0; q < 8; ++q) oth[q] = lds_rdf4(xa + 1024u * q);
    }
    SBAR();
    PVBLOCK();
    {
      const f32x16 sa = g ? o0 : o2, sb = g ? o1 : o3;
#pragma unroll
      for (int q = 0; q < 4; ++q) { lds_wrf4(xa + 1024u * q, quad(sa, q)); lds_wrf4(xa + 4096u + 1024u * q, quad(sb, q)); }
    }
    if (!g) { BAR_ONLY(); }
    lsum = swap32_sum(lsum);
    *(LAS f32x2*)(size_t)(L0 + 2u * PS + VOFF + 512u * w + 8u * lane) = (f32x2){mref, lsum};
    BAR_ONLY();
    STAMP(11);
    if (!g) {
#pragma unroll
      for (int q = 0; q < 8; ++q) oth[q] = lds_rdf4(xa + 1024u * q);
    }
    const f32x2 st = *(const LAS f32x2*)(size_t)(L0 + 2u * PS + VOFF + 512u * (w ^ 4u) + 8u * lane);
    const float mm = __builtin_fmaxf(mref, st[0]);
    const float fa = __builtin_amdgcn_exp2f(mref - mm), fb = __builtin_amdgcn_exp2f(st[0] - mm);
    const float inv = 1.0f / (fa * lsum + fb * st[1]);
    const float ca = fa * inv, cb = fb * inv;
    const f32x16 ka = g ? o2 : o0, kb = g ? o3 : o1;
#pragma unroll
    for (int d = 0; d < 2; ++d) {
#pragma unroll
      for (int sp = 0; sp < 2; ++sp) {
        const f32x4 u0 = oth[4 * d + 2 * sp], u1 = oth[4 * d + 2 * sp + 1];
        const f32x16& mo = d ? kb : ka;
        u32x4 r;
        r[0] = cvtpk(mo[8 * sp + 0] * ca + u0[0] * cb, mo[8 * sp + 1] * ca + u0[1] * cb);
        r[1] = cvtpk(mo[8 * sp + 2] * ca + u0[2] * cb, mo[8 * sp + 3] * ca + u0[3] * cb);
        r[2] = cvtpk(mo[8 * sp + 4] * ca + u1[0] * cb, mo[8 * sp + 5] * ca + u1[1] * cb);
        r[3] = cvtpk(mo[8 * sp + 6] * ca + u1[2] * cb, mo[8 * sp + 7] * ca + u1[3] * cb);
        own[2 * d + sp] = r;
      }
    }
  }
  const __amdgpu_buffer_rsrc_t srdF = __builtin_amdgcn_make_buffer_rsrc((void*)(ws + WS_OFRAG + (size_t)(b * NH + 8u * hg) * 32768u), (short)0, 8 * 32768, 0x00020000);
  const unsigned vfo = (2u * wq4 + g) * 4096u + 16u * lane;
#pragma unroll
  for (int k = 0; k < 4; ++k) __builtin_amdgcn_raw_buffer_store_b128(own[k], srdF, vfo + 1024u * k, im * 32768u, 16);
  WAIT_BAR(0);
  STAMP(12);
  GAS u64* flag1 = (GAS u64*)(ws + WS_FLAG1 + (b * 4u + hg) * 128u);
  if (tid == 0) __hip_atomic_store(flag1 + im, tag, __ATOMIC_RELAXED, __HIP_MEMORY_SCOPE_AGENT);
  WDMA4(6u + g, L0 + PS + g * VOFF);
  f32x16 c0 = {}, c1 = {};
  const unsigned ard = L0 + trl + 8192u * g;
#define UB(n) ((n) == 0 ? 2u * PS : (n) == 1 ? 3u * PS : (n) == 2 ? 4u * PS : (n) == 3 ? 4u * PS + VOFF : (n) == 4 ? 0u : (n) == 5 ? VOFF : (n) == 6 ? PS : PS + VOFF)
#define OSTEP(n, ks, bfr) do { \
    const bf16x8 a0_ = cat8(lds_rdtr(ard + UB(n) + 2048u * (ks)), lds_rdtr(ard + UB(n) + 2048u * (ks) + 1024u)); \
    const bf16x8 a1_ = cat8(lds_rdtr(ard + UB(n) + 2048u * (ks) + 512u), lds_rdtr(ard + UB(n) + 2048u * (ks) + 1536u)); \
    c0 = MFMA(a0_, __builtin_bit_cast(bf16x8, bfr), c0); c1 = MFMA(a1_, __builtin_bit_cast(bf16x8, bfr), c1); } while (0)
#pragma unroll
  for (int ks = 0; ks < 4; ++ks) OSTEP(0, ks, own[ks]);
  poll_flags(flag1 + (lane & 7u), tag, (lane & 7u) == im);
  STAMP(13);
  {
    u32x4 of[7][4];
#pragma unroll
    for (int n = 1; n < 8; ++n)
#pragma unroll
      for (int k = 0; k < 4; ++k) of[n - 1][k] = __builtin_amdgcn_raw_buffer_load_b128(srdF, vfo + 1024u * k, ((im + n) & 7u) * 32768u, 16);
    SBAR();
#pragma unroll
    for (int n = 1; n < 6; ++n)
#pragma unroll
      for (int ks = 0; ks < 4; ++ks) OSTEP(n, ks, of[n - 1][ks]);
    BAR_ONLY();
#pragma unroll
    for (int n = 6; n < 8; ++n)
#pragma unroll
      for (int ks = 0; ks < 4; ++ks) OSTEP(n, ks, of[n - 1][ks]);
  }
  STAMP(14);
  {
    const unsigned ra = L0 + (2u + g) * PS + VOFF + 4096u * wq4 + 16u * lane, rb = L0 + (3u - g) * PS + VOFF + 4096u * wq4 + 16u * lane;
    const f32x16 snd = g ? c0 : c1;
#pragma unroll
    for (int q = 0; q < 4; ++q) lds_wrf4(ra + 1024u * q, quad(snd, q));
    BAR_ONLY();
    f32x16 kp = g ? c1 : c0;
#pragma unroll
    for (int q = 0; q < 4; ++q) { const f32x4 u = lds_rdf4(rb + 1024u * q); kp[4 * q] += u[0]; kp[4 * q + 1] += u[1]; kp[4 * q + 2] += u[2]; kp[4 * q + 3] += u[3]; }
    const __amdgpu_buffer_rsrc_t srdP = __builtin_amdgcn_make_buffer_rsrc((void*)(ws + WS_PART + (size_t)(b * 8u + im) * 131072u), (short)0, 131072, 0x00020000);
    const unsigned vpo = g * 4096u + 16u * lane;
    if (wq4 != hg) {
#pragma unroll
      for (int q = 0; q < 4; ++q) __builtin_amdgcn_raw_buffer_store_b128(__builtin_bit_cast(u32x4, quad(kp, q)), srdP, vpo + 1024u * q, (wq4 * 4u + hg) * 8192u, 16);
    }
    WAIT_BAR(0);
    STAMP(15);
    GAS u64* flag2 = (GAS u64*)(ws + WS_FLAG2 + (b * 8u + im) * 128u);
    if (tid == 0) __hip_atomic_store(flag2 + hg, tag, __ATOMIC_RELAXED, __HIP_MEMORY_SCOPE_AGENT);
    if (wq4 == hg) {
      poll_flags(flag2 + (lane & 3u), tag, (lane & 3u) == hg);
      STAMP(16);
      u32x4 pp[3][4];
#pragma unroll
      for (int n = 1; n < 4; ++n)
#pragma unroll
        for (int q = 0; q < 4; ++q) pp[n - 1][q] = __builtin_amdgcn_raw_buffer_load_b128(srdP, vpo + 1024u * q, (hg * 4u + ((hg + n) & 3u)) * 8192u, 16);
      if (h == 0u && tid == 0u) *(GAS u64*)(ws + WS_EPOCH + 256u * b) = epoch + 1u;
      unsigned pk[8];
#pragma unroll
      for (int q = 0; q < 4; ++q) {
        f32x4 a = quad(kp, q);
#pragma unroll
        for (int n = 0; n < 3; ++n) a += __builtin_bit_cast(f32x4, pp[n][q]);
        pk[2 * q] = cvtpk(a[0], a[1]); pk[2 * q + 1] = cvtpk(a[2], a[3]);
      }
      char* dst = out + ((size_t)(b * SQ + 32u * hg + l32) * DM + 64u * im + 32u * g) * 2u + 16u * hi;
#pragma unroll
      for (int q = 0; q < 4; q += 2) {
        auto r0 = __builtin_amdgcn_permlane32_swap(pk[2 * q], pk[2 * q + 2], false, false);
        auto r1 = __builtin_amdgcn_permlane32_swap(pk[2 * q + 1], pk[2 * q + 3], false, false);
        *(u32x4*)(dst + 16 * q) = (u32x4){r0[0], r1[0], r0[1], r1[1]};
      }
    }
  }
}

extern "C" void kernel_launch(void* const* d_in, const int* in_sizes, int n_in, void* d_out, int out_size, void* d_ws, size_t ws_size, hipStream_t stream) {
  mha_kernel<<<256, 512, 0, stream>>>((const char*)d_in[0], (const char*)d_in[1], (const char*)d_in[2], (const char*)d_in[3], (const char*)d_in[4], (char*)d_out, (char*)d_ws + WS_BASE);
}
```

```cpp
#include <hip/hip_runtime.h>
#include <hip/hip_bf16.h>

typedef __bf16 bf16x8 __attribute__((ext_vector_type(8)));
typedef __bf16 bf16x4 __attribute__((ext_vector_type(4)));
typedef __bf16 bf16x2 __attribute__((ext_vector_type(2)));
typedef float f32x16 __attribute__((ext_vector_type(16)));
typedef float f32x4 __attribute__((ext_vector_type(4)));
typedef float f32x2 __attribute__((ext_vector_type(2)));
typedef unsigned u32x4 __attribute__((ext_vector_type(4)));
typedef unsigned long long u64;
#define LAS __attribute__((address_space(3)))
#define GAS __attribute__((address_space(1)))
#define DI __device__ __forceinline__
#define SBAR() __builtin_amdgcn_sched_barrier(0)
#define PIN(x) asm volatile("" : "+v"(x))
#define MFMA(a, b, c) __builtin_amdgcn_mfma_f32_32x32x16_bf16((a), (b), (c), 0, 0, 0)

#define PRIO_M 0
#define PRIO_S 1
#define XW_POL ""
#define WO_POL ""
#define WS_BASE 0u
constexpr int NB = 8, SQ = 128, DM = 512, NH = 32, SK = 2048, DH = 128;
constexpr unsigned PS = 32768u;
constexpr unsigned VOFF = 16384u;
constexpr float QSCALE = 0.08838834764831845f * 1.4426950408889634f;
constexpr float SUM_THR = 65536.0f;
constexpr unsigned WS_EPOCH = 0u, WS_FLAG1 = 4096u, WS_FLAG2 = 16384u, WS_OFRAG = 1u << 20, WS_PART = 16u << 20;
constexpr unsigned SPIN_CAP = 1u << 18;

DI unsigned rfl(unsigned x) { return __builtin_amdgcn_readfirstlane(x); }
DI unsigned cvtpk(float lo, float hi) { f32x2 v = {lo, hi}; bf16x2 b = __builtin_convertvector(v, bf16x2); return __builtin_bit_cast(unsigned, b); }
DI bf16x8 lds_rd128(unsigned addr) { return *(const LAS bf16x8*)(size_t)addr; }
DI f32x4 lds_rdf4(unsigned addr) { return *(const LAS f32x4*)(size_t)addr; }
DI bf16x4 lds_rdtr(unsigned addr) { return __builtin_amdgcn_ds_read_tr16_b64_v4bf16((LAS bf16x4*)(size_t)addr); }
DI bf16x8 cat8(bf16x4 a, bf16x4 b) { return __builtin_shufflevector(a, b, 0, 1, 2, 3, 4, 5, 6, 7); }
DI void lds_wr128(unsigned addr, u32x4 v) { *(LAS u32x4*)(size_t)addr = v; }
DI void lds_wrf4(unsigned addr, f32x4 v) { *(LAS f32x4*)(size_t)addr = v; }
DI float swap32_max(float v) { auto r = __builtin_amdgcn_permlane32_swap(__float_as_uint(v), __float_as_uint(v), false, false); return __builtin_fmaxf(__uint_as_float(r[0]), __uint_as_float(r[1])); }
DI float swap32_sum(float v) { auto r = __builtin_amdgcn_permlane32_swap(__float_as_uint(v), __float_as_uint(v), false, false); return __uint_as_float(r[0]) + __uint_as_float(r[1]); }
DI f32x4 quad(const f32x16& v, int q) { return (f32x4){v[4 * q], v[4 * q + 1], v[4 * q + 2], v[4 * q + 3]}; }
DI void poll_flags(const GAS u64* p, u64 tag, bool skip) {
  for (unsigned spin = 0; spin < SPIN_CAP; ++spin) {
    const u64 v = __hip_atomic_load(p, __ATOMIC_RELAXED, __HIP_MEMORY_SCOPE_AGENT);
    if (__all(skip || v == tag)) break;
    __builtin_amdgcn_s_sleep(2);
  }
  asm volatile("" ::: "memory");
}

#define DMA4(srd, voffA, voffB, soff, m0a) \
  asm volatile("s_mov_b32 m0, %0\n\ts_nop 0\n\t" \
               "buffer_load_dwordx4 %2, %4, %5 offen nt lds\n\t" \
               "buffer_load_dwordx4 %3, %4, %5 offen offset:2048 nt lds\n\t" \
               "s_mov_b32 m0, %1\n\ts_nop 0\n\t" \
               "buffer_load_dwordx4 %2, %4, %5 offen offset:128 nt lds\n\t" \
               "buffer_load_dwordx4 %3, %4, %5 offen offset:2176 nt lds" \
               :: "s"(m0a), "s"((m0a) + 896u), "v"(voffA), "v"(voffB), "s"(srd), "s"(soff) : "memory")
#define DMA1(pol, srd, voff, soff, m0a) \
  asm volatile("s_mov_b32 m0, %0\n\ts_nop 0\n\tbuffer_load_dwordx4 %1, %2, %3 offen" pol " lds" :: "s"(m0a), "v"(voff), "s"(srd), "s"(soff) : "memory")

#define STAMP(i)
#define STAMPC(i)
#define CYC(v)
#define BAR_ONLY() asm volatile("s_waitcnt lgkmcnt(0)\n\ts_barrier" ::: "memory")
#define WAIT_BAR(N) asm volatile("s_waitcnt vmcnt(" #N ") lgkmcnt(0)\n\ts_barrier" ::: "memory")

__global__ __launch_bounds__(512, 2) void mha_kernel(const char* __restrict__ x, const char* __restrict__ kc, const char* __restrict__ vc,
                                                     const char* __restrict__ wq, const char* __restrict__ wo, char* __restrict__ out, char* ws) {
  __shared__ __attribute__((aligned(1024))) char lds[163840];
  const unsigned tid = threadIdx.x, lane = tid & 63u, l32 = lane & 31u, hi = lane >> 5;
  const unsigned w = rfl(tid >> 6), wq4 = w & 3u, g = w >> 2;
  const unsigned blk = blockIdx.x, xcd = blk & 7u, j = blk >> 3;
  const unsigned b = 4u * (xcd & 1u) + (j >> 3), hg = xcd >> 1, im = j & 7u, h = 8u * hg + im;
  const unsigned bh = b * NH + h;
  const unsigned L0 = (unsigned)(size_t)lds;
  STAMP(0); STAMPC(19);

  const __amdgpu_buffer_rsrc_t srdX = __builtin_amdgcn_make_buffer_rsrc((void*)(x + (size_t)b * (SQ * DM * 2)), (short)0, SQ * DM * 2, 0x00020000);
  const __amdgpu_buffer_rsrc_t srdW = __builtin_amdgcn_make_buffer_rsrc((void*)(wq + (size_t)h * 256), (short)0, (int)(DM * 8192u - h * 256u), 0x00020000);
  const __amdgpu_buffer_rsrc_t srdK = __builtin_amdgcn_make_buffer_rsrc((void*)(kc + (size_t)bh * (SK * 256)), (short)0, SK * 256, 0x00020000);
  const __amdgpu_buffer_rsrc_t srdV = __builtin_amdgcn_make_buffer_rsrc((void*)(vc + (size_t)bh * (SK * 256)), (short)0, SK * 256, 0x00020000);

  const unsigned sub = lane >> 5, r8 = (lane >> 2) & 7u, slot = lane & 3u;
  const unsigned vx0 = (16u * w + r8) * 1024u + 64u * sub + 16u * (slot ^ (r8 >> 2));
  const unsigned vx1 = (16u * w + 8u + r8) * 1024u + 64u * sub + 16u * (slot ^ (2u + (r8 >> 2)));
  const unsigned vw = (8u * w + r8) * 8192u + 64u * sub + 16u * slot;
  const unsigned crot = h + 2u * b;
#define XB(c) (L0 + ((c) % 5 == 0 ? 3u * PS : (c) % 5 == 1 ? VOFF : (c) % 5 == 2 ? 4u * PS : (c) % 5 == 3 ? 0u : 2u * PS))
#define WB(c) (L0 + ((c) % 5 == 0 ? 3u * PS + VOFF : (c) % 5 == 1 ? PS + VOFF : (c) % 5 == 2 ? 4u * PS + VOFF : (c) % 5 == 3 ? PS : 2u * PS + VOFF))
#define QL(c, k) do { const unsigned cc_ = ((c) + crot) & 7u; \
    if ((k) == 0) DMA1(XW_POL, srdX, vx0, 128u * cc_, XB(c) + 2048u * w); \
    else if ((k) == 1) DMA1(XW_POL, srdX, vx1, 128u * cc_, XB(c) + 2048u * w + 1024u); \
    else if ((k) == 2) DMA1(XW_POL, srdW, vw, 524288u * cc_, WB(c) + 2048u * w); \
    else DMA1(XW_POL, srdW, vw, 524288u * cc_ + 128u, WB(c) + 2048u * w + 1024u); } while (0)
#define QLOAD(c) do { QL(c, 0); QL(c, 1); QL(c, 2); QL(c, 3); } while (0)
  QLOAD(0);
  asm volatile("s_barrier" ::: "memory");
  QLOAD(1);
  u64 epoch;
  asm volatile("s_load_dwordx2 %0, %1, 0x0\n\ts_waitcnt lgkmcnt(0)" : "=s"(epoch) : "s"(ws + WS_EPOCH + 256u * b) : "memory");
  const u64 tag = epoch * 0x9E3779B97F4A7C15ull + 0xD1B54A32D192ED03ull;
  const unsigned vkA = r8 * 256u + 64u * sub + 16u * (slot ^ (r8 >> 2));
  const unsigned vkB = r8 * 256u + 64u * sub + 16u * (slot ^ (2u + (r8 >> 2)));
  const unsigned vvA = r8 * 256u + 64u * sub + 16u * slot;
  const unsigned wrow = wq4 * 4096u;
#define DA() DMA4(srdK, vkA, vkB, (g ^ 1u) * 16384u + wrow, L0 + (g ^ 1u) * PS + wrow)
#define DB() do { if (g) DMA4(srdK, vkA, vkB, 2u * 16384u + wrow, L0 + 2u * PS + wrow); else DMA4(srdV, vvA, vvA, wrow, L0 + 2u * PS + VOFF + wrow); } while (0)
#define DC() WDMA4(g, L0 + 3u * PS + g * VOFF)
  const __amdgpu_buffer_rsrc_t srdO = __builtin_amdgcn_make_buffer_rsrc((void*)(wo + 128u * im), (short)0, (int)(4096u * 1024u - 128u * im), 0x00020000);
  const unsigned vwo = r8 * 1024u + 64u * sub + 16u * slot;
#define WDMA4(n, ldsa) do { const unsigned so_ = ((8u * hg + ((im + (n)) & 7u)) * 128u + 32u * wq4) * 1024u; \
    DMA1(WO_POL, srdO, vwo, so_, (ldsa) + wrow); DMA1(WO_POL, srdO, vwo, so_ + 8192u, (ldsa) + wrow + 1024u); \
    DMA1(WO_POL, srdO, vwo, so_ + 16384u, (ldsa) + wrow + 2048u); DMA1(WO_POL, srdO, vwo, so_ + 24576u, (ldsa) + wrow + 3072u); } while (0)

  STAMP(1);
  bf16x8 qf[8];
  {
    f32x16 qa0 = {}, qa1 = {};
    const unsigned xrd0 = 4096u * wq4 + 1024u * (l32 >> 3) + 64u * (l32 & 7u) + 16u * ((0u + hi) ^ ((l32 >> 2) & 3u));
    const unsigned xrd1 = 4096u * wq4 + 1024u * (l32 >> 3) + 64u * (l32 & 7u) + 16u * ((2u + hi) ^ ((l32 >> 2) & 3u));
    const unsigned p = lane & 3u, psw = ((p & 1u) << 1) | (p >> 1);
    const unsigned wrd = 2048u * hi + 64u * ((lane >> 2) & 3u) + 32u * ((lane >> 4) & 1u) + 8u * psw + 1024u * g;
    bf16x8 fx[2][4], fa[2][4], fb[2][4];
#define QREAD1(c, s) do { \
      fx[(c) & 1][s] = lds_rd128(XB(c) + ((s & 1) ? xrd1 : xrd0) + 512u * (s >> 1)); \
      fa[(c) & 1][s] = cat8(lds_rdtr(WB(c) + wrd + 4096u * s), lds_rdtr(WB(c) + wrd + 4096u * s + 256u)); \
      fb[(c) & 1][s] = cat8(lds_rdtr(WB(c) + wrd + 4096u * s + 512u), lds_rdtr(WB(c) + wrd + 4096u * s + 768u)); SBAR(); } while (0)
#define QMM(c, s) do { qa0 = MFMA(fa[(c) & 1][s], fx[(c) & 1][s], qa0); qa1 = MFMA(fb[(c) & 1][s], fx[(c) & 1][s], qa1); SBAR(); } while (0)
#define QSUB(c, s, LD) do { QMM((c) - 1, s); if (LD) { QL((c) + 3, s); SBAR(); } QREAD1(c, s); } while (0)
#define QSTEP(c, N, LD) do { WAIT_BAR(N); QSUB(c, 0, LD); QSUB(c, 1, LD); QSUB(c, 2, LD); QSUB(c, 3, LD); } while (0)
    WAIT_BAR(4); QREAD1(0, 0); QREAD1(0, 1); QREAD1(0, 2); QREAD1(0, 3); QLOAD(2); QLOAD(3); STAMP(2);
    QSTEP(1, 8, 1);
    QSTEP(2, 8, 1);
    QSTEP(3, 8, 1); STAMP(3);
    QSTEP(4, 8, 1); DA();
    QSTEP(5, 12, 0);
    QSTEP(6, 8, 0);
    QSTEP(7, 4, 0);
    QMM(7, 0); QMM(7, 1); QMM(7, 2); QMM(7, 3); DB(); STAMP(4);
    u32x4 mine[4];
#pragma unroll
    for (int sp = 0; sp < 2; ++sp) {
#pragma unroll
      for (int e = 0; e < 4; ++e) {
        mine[sp][e] = cvtpk(qa0[8 * sp + 2 * e] * QSCALE, qa0[8 * sp + 2 * e + 1] * QSCALE);
        mine[2 + sp][e] = cvtpk(qa1[8 * sp + 2 * e] * QSCALE, qa1[8 * sp + 2 * e + 1] * QSCALE);
      }
    }
    const unsigned qx_w = L0 + VOFF + g * PS + 4096u * wq4 + 16u * lane, qx_r = L0 + VOFF + (g ^ 1u) * PS + 4096u * wq4 + 16u * lane;
#pragma unroll
    for (int i = 0; i < 4; ++i) lds_wr128(qx_w + 1024u * i, mine[i]);
    WAIT_BAR(4);
#pragma unroll
    for (int i = 0; i < 4; ++i) {
      const bf16x8 oth = lds_rd128(qx_r + 1024u * i);
      const bf16x8 own = __builtin_bit_cast(bf16x8, mine[i]);
      qf[i] = g ? oth : own; qf[4 + i] = g ? own : oth;
    }
  }
  STAMP(5); STAMPC(20);
  const unsigned krd0 = L0 + 2048u * (l32 >> 3) + 64u * (l32 & 7u) + 16u * ((0u + hi) ^ ((l32 >> 2) & 3u));
  const unsigned krd1 = L0 + 2048u * (l32 >> 3) + 64u * (l32 & 7u) + 16u * ((2u + hi) ^ ((l32 >> 2) & 3u));
  const unsigned trl = 8u * (lane & 3u) + 64u * ((lane >> 2) & 3u) + 32u * ((lane >> 4) & 1u) + 256u * hi;
  const unsigned vrd = L0 + VOFF + trl;
  f32x16 o0 = {}, o1 = {}, o2 = {}, o3 = {}, s0, s1, negm = {};
  PIN(negm);
  float mref = 0.f, lsum = 0.f;
  float one = 1.0f; PIN(one);
  u32x4 pf[4] = {};
  bf16x8 fr[8];
#define KFR(m, sl) lds_rd128((((m) >> 1) & 1 ? krd1 : krd0) + (sl) + 8192u * ((m) & 1) + 512u * ((m) >> 2))
#define VFR(m, sl) cat8(lds_rdtr(vrd + (sl) + 4096u * ((m) >> 2) + 512u * ((m) & 3)), lds_rdtr(vrd + (sl) + 4096u * ((m) >> 2) + 512u * ((m) & 3) + 2048u))
#define PVBLOCK() do { _Pragma("unroll") for (int m = 0; m < 16; ++m) { \
      if (m + LA < 16) fr[(m + LA) & 7] = VFR(m + LA, sl); \
      const bf16x8 pb = __builtin_bit_cast(bf16x8, pf[m >> 2]); \
      if ((m & 3) == 0) o0 = MFMA(fr[m & 7], pb, o0); \
      else if ((m & 3) == 1) o1 = MFMA(fr[m & 7], pb, o1); \
      else if ((m & 3) == 2) o2 = MFMA(fr[m & 7], pb, o2); \
      else o3 = MFMA(fr[m & 7], pb, o3); \
      SBAR(); } } while (0)
  constexpr int LA = 6;
  unsigned sl = g * PS;
  if (g) {
    WDMA4(0u, L0 + 3u * PS);
    BAR_ONLY();
  }
  u64 cy0 = 0, cy1 = 0, cy2 = 0, cy3 = 0, cy4 = 0, cy5 = 0, sg[5] = {};
  for (unsigned t = 0; t < 16; ++t) {
    const unsigned i = 2u * t + g;
    CYC(cy0);
    __builtin_amdgcn_s_setprio(PRIO_M);
#pragma unroll
    for (int m = 0; m < LA; ++m) fr[m] = KFR(m, sl);
    SBAR();
#pragma unroll
    for (int m = 0; m < 16; ++m) {
      if (m + LA < 16) fr[(m + LA) & 7] = KFR(m + LA, sl);
      else fr[(m + LA) & 7] = VFR(m + LA - 16, sl);
      if (m == 0) s0 = MFMA(fr[0], qf[0], negm);
      else if (m == 1) s1 = MFMA(fr[1], qf[0], negm);
      else if (m & 1) s1 = MFMA(fr[m & 7], qf[m >> 1], s1);
      else s0 = MFMA(fr[m & 7], qf[m >> 1], s0);
      SBAR();
    }
    if (t > 0) PVBLOCK();
    __builtin_amdgcn_s_setprio(PRIO_S);
    CYC(cy1);
    if (i == 31u) { WAIT_BAR(4); } else { WAIT_BAR(0); }
    CYC(cy2);
    const unsigned pn = i + 3u;
    if (pn <= 31u) DMA4(srdK, vkA, vkB, pn * 16384u + wrow, L0 + sl + wrow);
    if (pn <= 33u) DMA4(srdV, vvA, vvA, (pn - 2u) * 16384u + wrow, L0 + sl + VOFF + wrow);
    if (pn == 32u || pn == 33u) WDMA4(pn - 28u, L0 + sl);
    if (i >= 4u && i < 16u) {
      const unsigned idx = 4u * i + wq4;
      DMA1(WO_POL, srdO, vwo, ((8u * hg + ((im + (idx >> 4)) & 7u)) * 128u + 8u * (idx & 15u)) * 1024u, L0 + 3u * PS + 1024u * idx);
    }
    sl += 2u * PS; if (sl >= 3u * PS) sl -= 3u * PS;
    CYC(cy3);
    float ts;
#define EXPS() do { float a0 = 0.f, a1 = 0.f; \
      _Pragma("unroll") for (int e = 0; e < 8; ++e) { const float e0 = __builtin_amdgcn_exp2f(s0[2 * e]), e1 = __builtin_amdgcn_exp2f(s0[2 * e + 1]); a0 += e0; a1 = __builtin_fmaf(e1, one, a1); pf[e >> 2][e & 3] = cvtpk(e0, e1); } \
      _Pragma("unroll") for (int e = 0; e < 8; ++e) { const float e0 = __builtin_amdgcn_exp2f(s1[2 * e]), e1 = __builtin_amdgcn_exp2f(s1[2 * e + 1]); a0 += e0; a1 = __builtin_fmaf(e1, one, a1); pf[2 + (e >> 2)][e & 3] = cvtpk(e0, e1); } \
      ts = a0 + a1; } while (0)
    bool redo = (t == 0);
    if (!redo) { EXPS(); redo = __any(!(ts <= SUM_THR)); }
    if (__builtin_expect(redo, 0)) {
      float mx = s0[0];
#pragma unroll
      for (int e = 1; e < 16; ++e) mx = __builtin_fmaxf(mx, s0[e]);
#pragma unroll
      for (int e = 0; e < 16; ++e) mx = __builtin_fmaxf(mx, s1[e]);
      mx = swap32_max(mx);
      const float dl = (t == 0) ? mx : __builtin_fmaxf(mx, 0.f);
      const float f = (t == 0) ? 1.f : __builtin_amdgcn_exp2f(-dl);
      mref += dl; lsum *= f;
#pragma unroll
      for (int e = 0; e < 16; ++e) { s0[e] -= dl; s1[e] -= dl; o0[e] *= f; o1[e] *= f; o2[e] *= f; o3[e] *= f; negm[e] = -mref; }
      PIN(negm);
      EXPS();
    }
    lsum += ts;
    PIN(pf[0]); PIN(pf[1]); PIN(pf[2]); PIN(pf[3]); PIN(lsum);
    SBAR();
    CYC(cy4);
    BAR_ONLY();
    CYC(cy5);
    if (t >= 2 && t < 12) { sg[0] += cy1 - cy0; sg[1] += cy2 - cy1; sg[2] += cy3 - cy2; sg[3] += cy4 - cy3; sg[4] += cy5 - cy4; }
  }
  __builtin_amdgcn_s_setprio(0);
  STAMP(10); STAMPC(21);
  const __amdgpu_buffer_rsrc_t srdF = __builtin_amdgcn_make_buffer_rsrc((void*)(ws + WS_OFRAG + (size_t)(b * NH + 8u * hg) * 32768u), (short)0, 8 * 32768, 0x00020000);
  const unsigned vfo = (2u * wq4 + g) * 4096u + 16u * lane;
  GAS u64* flag1 = (GAS u64*)(ws + WS_FLAG1 + (b * 4u + hg) * 128u);
  const GAS u64* fpoll = flag1 + ((im + lane) & 7u);
#define POLL1() ((unsigned)__ballot(__hip_atomic_load(fpoll, __ATOMIC_RELAXED, __HIP_MEMORY_SCOPE_AGENT) == tag) & 0xFEu)
#define OLOAD(B, n) do { const unsigned so_ = ((im + (n)) & 7u) * 32768u, vo_ = vfo + (((n) & 8u) << 24);     \
    _Pragma("unroll") for (int k_ = 0; k_ < 4; ++k_) B[k_] = __builtin_amdgcn_raw_buffer_load_b128(srdF, vo_ + 1024u * k_, so_, 16); SBAR(); } while (0)
  u32x4 own[4], b0[4], b1[4], b2[4];
  unsigned rdy, rem = 0xFEu, id0 = 8u, id1 = 8u, id2;
  {
    const unsigned xa = L0 + PS + 8192u * wq4 + 16u * lane;
    f32x4 oth[8];
    u64 fv = __hip_atomic_load(fpoll, __ATOMIC_RELAXED, __HIP_MEMORY_SCOPE_AGENT);
    SBAR();
    if (g) {
#pragma unroll
      for (int q = 0; q < 8; ++q) oth[q] = lds_rdf4(xa + 1024u * q);
    }
#pragma unroll
    for (int m = 0; m < LA; ++m) fr[m] = VFR(m, sl);
    SBAR();
    PVBLOCK();
    {
      const f32x16 sa = g ? o0 : o2, sb = g ? o1 : o3;
#pragma unroll
      for (int q = 0; q < 4; ++q) { lds_wrf4(xa + 1024u * q, quad(sa, q)); lds_wrf4(xa + 4096u + 1024u * q, quad(sb, q)); }
    }
    if (!g) {
      WAIT_BAR(5);
      fv = __hip_atomic_load(fpoll, __ATOMIC_RELAXED, __HIP_MEMORY_SCOPE_AGENT);
    }
    lsum = swap32_sum(lsum);
    *(LAS f32x2*)(size_t)(L0 + 2u * PS + VOFF + 512u * w + 8u * lane) = (f32x2){mref, lsum};
    rdy = (unsigned)__ballot(fv == tag) & 0xFEu;
    asm volatile("" ::: "memory");
    if (__builtin_popcount(rdy & 0x3Eu) >= 2) {
      id0 = __builtin_ctz(rdy); rem &= ~(1u << id0);
      id1 = __builtin_ctz(rdy & rem); rem &= ~(1u << id1);
    }
    OLOAD(b0, id0); OLOAD(b1, id1);
    BAR_ONLY();
    STAMP(11);
    if (!g) {
#pragma unroll
      for (int q = 0; q < 8; ++q) oth[q] = lds_rdf4(xa + 1024u * q);
    }
    const f32x2 st = *(const LAS f32x2*)(size_t)(L0 + 2u * PS + VOFF + 512u * (w ^ 4u) + 8u * lane);
    const float mm = __builtin_fmaxf(mref, st[0]);
    const float fa = __builtin_amdgcn_exp2f(mref - mm), fb = __builtin_amdgcn_exp2f(st[0] - mm);
    const float inv = 1.0f / (fa * lsum + fb * st[1]);
    const float ca = fa * inv, cb = fb * inv;
    const f32x16 ka = g ? o2 : o0, kb = g ? o3 : o1;
#pragma unroll
    for (int d = 0; d < 2; ++d) {
#pragma unroll
      for (int sp = 0; sp < 2; ++sp) {
        const f32x4 u0 = oth[4 * d + 2 * sp], u1 = oth[4 * d + 2 * sp + 1];
        const f32x16& mo = d ? kb : ka;
        u32x4 r;
        r[0] = cvtpk(mo[8 * sp + 0] * ca + u0[0] * cb, mo[8 * sp + 1] * ca + u0[1] * cb);
        r[1] = cvtpk(mo[8 * sp + 2] * ca + u0[2] * cb, mo[8 * sp + 3] * ca + u0[3] * cb);
        r[2] = cvtpk(mo[8 * sp + 4] * ca + u1[0] * cb, mo[8 * sp + 5] * ca + u1[1] * cb);
        r[3] = cvtpk(mo[8 * sp + 6] * ca + u1[2] * cb, mo[8 * sp + 7] * ca + u1[3] * cb);
        own[2 * d + sp] = r;
      }
    }
  }
#pragma unroll
  for (int k = 0; k < 4; ++k) __builtin_amdgcn_raw_buffer_store_b128(own[k], srdF, vfo + 1024u * k, im * 32768u, 16);
  SBAR();
  PIN(b0[0]); PIN(b0[1]); PIN(b0[2]); PIN(b0[3]);
  if (!g) {
    const unsigned so = ((8u * hg + ((im + 6u + (wq4 >> 1)) & 7u)) * 128u + 64u * (wq4 & 1u)) * 1024u;
#pragma unroll
    for (int q = 0; q < 8; ++q) DMA1(WO_POL, srdO, vwo, so + 8192u * q, L0 + PS + 8192u * wq4 + 1024u * q);
  }
  f32x16 c0 = {}, c1 = {};
  const unsigned ard = L0 + trl + 8192u * g;
  bf16x8 aa[8], ab[8];
#define ARD1(A, ua_, ks) do { A[2 * (ks)] = cat8(lds_rdtr(ua_ + 2048u * (ks)), lds_rdtr(ua_ + 2048u * (ks) + 1024u)); \
      A[2 * (ks) + 1] = cat8(lds_rdtr(ua_ + 2048u * (ks) + 512u), lds_rdtr(ua_ + 2048u * (ks) + 1536u)); } while (0)
#define UA(n) (ard + (((0x32049876u >> (4u * ((n) & 7u))) & 15u) << 14))
#define AREAD(A, n) do { const unsigned ua_ = UA(n); ARD1(A, ua_, 0); ARD1(A, ua_, 1); ARD1(A, ua_, 2); ARD1(A, ua_, 3); SBAR(); } while (0)
#define PROC(A, B, AN, nn) do { const unsigned un_ = UA(nn); \
    _Pragma("unroll") for (int ks = 0; ks < 4; ++ks) { \
      c0 = MFMA(A[2 * ks], __builtin_bit_cast(bf16x8, B[ks]), c0); c1 = MFMA(A[2 * ks + 1], __builtin_bit_cast(bf16x8, B[ks]), c1); SBAR(); \
      ARD1(AN, un_, ks); SBAR(); } } while (0)
#define PROCL(A, B) do { _Pragma("unroll") for (int ks = 0; ks < 4; ++ks) { \
      c0 = MFMA(A[2 * ks], __builtin_bit_cast(bf16x8, B[ks]), c0); c1 = MFMA(A[2 * ks + 1], __builtin_bit_cast(bf16x8, B[ks]), c1); } SBAR(); } while (0)
#define PICK(id) do { unsigned spin_ = 0; \
    while ((rdy & rem) == 0u && ++spin_ < SPIN_CAP) { rdy = POLL1(); if (rdy & rem) break; __builtin_amdgcn_s_sleep(2); } \
    asm volatile("" ::: "memory"); \
    id = (rdy & rem) ? __builtin_ctz(rdy & rem) : __builtin_ctz(rem); rem &= ~(1u << id); } while (0)
  AREAD(aa, 0u);
  const bool early = id0 < 8u;
  PROC(aa, own, ab, id0);
  if (early) { PROC(ab, b0, aa, id1); }
  WAIT_BAR(0);
  STAMP(12);
  if (tid == 0) __hip_atomic_store(flag1 + im, tag, __ATOMIC_RELAXED, __HIP_MEMORY_SCOPE_AGENT);
  if (!early) {
    PICK(id0); OLOAD(b0, id0); AREAD(ab, id0);
    PICK(id1); OLOAD(b1, id1);
    PROC(ab, b0, aa, id1);
  }
  STAMP(13);
  PICK(id2); OLOAD(b2, id2);
  PICK(id0); OLOAD(b0, id0);
  PROC(aa, b1, ab, id2);
  PICK(id1); OLOAD(b1, id1);
  PROC(ab, b2, aa, id0);
  PICK(id2); OLOAD(b2, id2);
  PROC(aa, b0, ab, id1);
  PICK(id0); OLOAD(b0, id0);
  PROC(ab, b1, aa, id2);
  PROC(aa, b2, ab, id0);
  PROCL(ab, b0);
  STAMP(14);
  {
    const unsigned ra = L0 + 2u * g * PS + VOFF + 4096u * wq4 + 16u * lane, rb = L0 + 2u * (g ^ 1u) * PS + VOFF + 4096u * wq4 + 16u * lane;
    const f32x16 snd = g ? c0 : c1;
#pragma unroll
    for (int q = 0; q < 4; ++q) lds_wrf4(ra + 1024u * q, quad(snd, q));
    BAR_ONLY();
    f32x16 kp = g ? c1 : c0;
#pragma unroll
    for (int q = 0; q < 4; ++q) { const f32x4 u = lds_rdf4(rb + 1024u * q); kp[4 * q] += u[0]; kp[4 * q + 1] += u[1]; kp[4 * q + 2] += u[2]; kp[4 * q + 3] += u[3]; }
    const __amdgpu_buffer_rsrc_t srdP = __builtin_amdgcn_make_buffer_rsrc((void*)(ws + WS_PART + (size_t)(b * 8u + im) * 131072u), (short)0, 131072, 0x00020000);
    const unsigned vpo = g * 4096u + 16u * lane;
    if (wq4 != hg) {
#pragma unroll
      for (int q = 0; q < 4; ++q) __builtin_amdgcn_raw_buffer_store_b128(__builtin_bit_cast(u32x4, quad(kp, q)), srdP, vpo + 1024u * q, (wq4 * 4u + hg) * 8192u, 16);
    }
    WAIT_BAR(0);
    STAMP(15);
    GAS u64* flag2 = (GAS u64*)(ws + WS_FLAG2 + (b * 8u + im) * 128u);
    if (tid == 0) __hip_atomic_store(flag2 + hg, tag, __ATOMIC_RELAXED, __HIP_MEMORY_SCOPE_AGENT);
    if (wq4 == hg) {
      poll_flags(flag2 + (lane & 3u), tag, (lane & 3u) == hg);
      STAMP(16);
      u32x4 pp[3][4];
#pragma unroll
      for (int n = 1; n < 4; ++n)
#pragma unroll
        for (int q = 0; q < 4; ++q) pp[n - 1][q] = __builtin_amdgcn_raw_buffer_load_b128(srdP, vpo + 1024u * q, (hg * 4u + ((hg + n) & 3u)) * 8192u, 16);
      if (h == 0u && tid == 0u) *(GAS u64*)(ws + WS_EPOCH + 256u * b) = epoch + 1u;
      unsigned pk[8];
#pragma unroll
      for (int q = 0; q < 4; ++q) {
        f32x4 a = quad(kp, q);
#pragma unroll
        for (int n = 0; n < 3; ++n) a += __builtin_bit_cast(f32x4, pp[n][q]);
        pk[2 * q] = cvtpk(a[0], a[1]); pk[2 * q + 1] = cvtpk(a[2], a[3]);
      }
      char* dst = out + ((size_t)(b * SQ + 32u * hg + l32) * DM + 64u * im + 32u * g) * 2u + 16u * hi;
#pragma unroll
      for (int q = 0; q < 4; q += 2) {
        auto r0 = __builtin_amdgcn_permlane32_swap(pk[2 * q], pk[2 * q + 2], false, false);
        auto r1 = __builtin_amdgcn_permlane32_swap(pk[2 * q + 1], pk[2 * q + 3], false, false);
        *(u32x4*)(dst + 16 * q) = (u32x4){r0[0], r1[0], r0[1], r1[1]};
      }
    }
  }
}

extern "C" void kernel_launch(void* const* d_in, const int* in_sizes, int n_in, void* d_out, int out_size, void* d_ws, size_t ws_size, hipStream_t stream) {
  mha_kernel<<<256, 512, 0, stream>>>((const char*)d_in[0], (const char*)d_in[1], (const char*)d_in[2], (const char*)d_in[3], (const char*)d_in[4], (char*)d_out, (char*)d_ws + WS_BASE);
}
```

```cpp
#include <hip/hip_runtime.h>
#include <hip/hip_bf16.h>
#include <cstdio>

typedef __bf16 bf16x8 __attribute__((ext_vector_type(8)));
typedef __bf16 bf16x4 __attribute__((ext_vector_type(4)));
typedef __bf16 bf16x2 __attribute__((ext_vector_type(2)));
typedef float f32x16 __attribute__((ext_vector_type(16)));
typedef float f32x4 __attribute__((ext_vector_type(4)));
typedef float f32x2 __attribute__((ext_vector_type(2)));
typedef unsigned u32x4 __attribute__((ext_vector_type(4)));
typedef unsigned long long u64;
#define LAS __attribute__((address_space(3)))
#define GAS __attribute__((address_space(1)))
#define DI __device__ __forceinline__
#define SBAR() __builtin_amdgcn_sched_barrier(0)
#define PIN(x) asm volatile("" : "+v"(x))
#define MFMA(a, b, c) __builtin_amdgcn_mfma_f32_32x32x16_bf16((a), (b), (c), 0, 0, 0)

#define PRIO_M 0
#define PRIO_S 1
#define SELFWARM 0
#define XW_POL ""
#define WO_POL ""
#define WS_BASE 0u
constexpr int NB = 8, SQ = 128, DM = 512, NH = 32, SK = 2048, DH = 128;
constexpr unsigned PS = 32768u;
constexpr unsigned VOFF = 16384u;
constexpr float QSCALE = 0.08838834764831845f * 1.4426950408889634f;
constexpr float SUM_THR = 65536.0f;
constexpr unsigned WS_EPOCH = 0u, WS_FLAG1 = 4096u, WS_FLAG2 = 16384u, WS_OFRAG = 1u << 20, WS_PART = 16u << 20;
constexpr unsigned SPIN_CAP = 1u << 18;

DI unsigned rfl(unsigned x) { return __builtin_amdgcn_readfirstlane(x); }
DI unsigned cvtpk(float lo, float hi) { f32x2 v = {lo, hi}; bf16x2 b = __builtin_convertvector(v, bf16x2); return __builtin_bit_cast(unsigned, b); }
DI bf16x8 lds_rd128(unsigned addr) { return *(const LAS bf16x8*)(size_t)addr; }
DI f32x4 lds_rdf4(unsigned addr) { return *(const LAS f32x4*)(size_t)addr; }
DI bf16x4 lds_rdtr(unsigned addr) { return __builtin_amdgcn_ds_read_tr16_b64_v4bf16((LAS bf16x4*)(size_t)addr); }
DI bf16x8 cat8(bf16x4 a, bf16x4 b) { return __builtin_shufflevector(a, b, 0, 1, 2, 3, 4, 5, 6, 7); }
DI void lds_wr128(unsigned addr, u32x4 v) { *(LAS u32x4*)(size_t)addr = v; }
DI void lds_wrf4(unsigned addr, f32x4 v) { *(LAS f32x4*)(size_t)addr = v; }
DI float swap32_max(float v) { auto r = __builtin_amdgcn_permlane32_swap(__float_as_uint(v), __float_as_uint(v), false, false); return __builtin_fmaxf(__uint_as_float(r[0]), __uint_as_float(r[1])); }
DI float swap32_sum(float v) { auto r = __builtin_amdgcn_permlane32_swap(__float_as_uint(v), __float_as_uint(v), false, false); return __uint_as_float(r[0]) + __uint_as_float(r[1]); }
DI f32x4 quad(const f32x16& v, int q) { return (f32x4){v[4 * q], v[4 * q + 1], v[4 * q + 2], v[4 * q + 3]}; }
DI void poll_flags(const GAS u64* p, u64 tag, bool skip) {
  for (unsigned spin = 0; spin < SPIN_CAP; ++spin) {
    const u64 v = __hip_atomic_load(p, __ATOMIC_RELAXED, __HIP_MEMORY_SCOPE_AGENT);
    if (__all(skip || v == tag)) break;
    __builtin_amdgcn_s_sleep(2);
  }
  asm volatile("" ::: "memory");
}

#define DMA4(srd, voffA, voffB, soff, m0a) \
  asm volatile("s_mov_b32 m0, %0\n\ts_nop 0\n\t" \
               "buffer_load_dwordx4 %2, %4, %5 offen nt lds\n\t" \
               "buffer_load_dwordx4 %3, %4, %5 offen offset:2048 nt lds\n\t" \
               "s_mov_b32 m0, %1\n\ts_nop 0\n\t" \
               "buffer_load_dwordx4 %2, %4, %5 offen offset:128 nt lds\n\t" \
               "buffer_load_dwordx4 %3, %4, %5 offen offset:2176 nt lds" \
               :: "s"(m0a), "s"((m0a) + 896u), "v"(voffA), "v"(voffB), "s"(srd), "s"(soff) : "memory")
#define DMA1(pol, srd, voff, soff, m0a) \
  asm volatile("s_mov_b32 m0, %0\n\ts_nop 0\n\tbuffer_load_dwordx4 %1, %2, %3 offen" pol " lds" :: "s"(m0a), "v"(voff), "s"(srd), "s"(soff) : "memory")

#define STAMP(i)
#define STAMPC(i)
#define CYC(v)
#define BAR_ONLY() asm volatile("s_waitcnt lgkmcnt(0)\n\ts_barrier" ::: "memory")
#define WAIT_BAR(N) asm volatile("s_waitcnt vmcnt(" #N ") lgkmcnt(0)\n\ts_barrier" ::: "memory")

__global__ __launch_bounds__(512, 2) void mha_kernel(const char* __restrict__ x, const char* __restrict__ kc, const char* __restrict__ vc,
                                                     const char* __restrict__ wq, const char* __restrict__ wo, char* __restrict__ out, char* ws) {
  __shared__ __attribute__((aligned(1024))) char lds[163840];
  const unsigned tid = threadIdx.x, lane = tid & 63u, l32 = lane & 31u, hi = lane >> 5;
  const unsigned w = rfl(tid >> 6), wq4 = w & 3u, g = w >> 2;
  const unsigned blk = blockIdx.x, xcd = blk & 7u, j = blk >> 3;
  const unsigned b = 4u * (xcd & 1u) + (j >> 3), hg = xcd >> 1, im = j & 7u, h = 8u * hg + im;
  const unsigned bh = b * NH + h;
  const unsigned L0 = (unsigned)(size_t)lds;
  STAMP(0); STAMPC(19);

  const __amdgpu_buffer_rsrc_t srdX = __builtin_amdgcn_make_buffer_rsrc((void*)(x + (size_t)b * (SQ * DM * 2)), (short)0, SQ * DM * 2, 0x00020000);
  const __amdgpu_buffer_rsrc_t srdW = __builtin_amdgcn_make_buffer_rsrc((void*)(wq + (size_t)h * 256), (short)0, (int)(DM * 8192u - h * 256u), 0x00020000);
  const __amdgpu_buffer_rsrc_t srdK = __builtin_amdgcn_make_buffer_rsrc((void*)(kc + (size_t)bh * (SK * 256)), (short)0, SK * 256, 0x00020000);
  const __amdgpu_buffer_rsrc_t srdV = __builtin_amdgcn_make_buffer_rsrc((void*)(vc + (size_t)bh * (SK * 256)), (short)0, SK * 256, 0x00020000);

  const unsigned sub = lane >> 5, r8 = (lane >> 2) & 7u, slot = lane & 3u;
  const unsigned vx0 = (16u * w + r8) * 1024u + 64u * sub + 16u * (slot ^ (r8 >> 2));
  const unsigned vx1 = (16u * w + 8u + r8) * 1024u + 64u * sub + 16u * (slot ^ (2u + (r8 >> 2)));
  const unsigned vw = (8u * w + r8) * 8192u + 64u * sub + 16u * slot;
  const unsigned crot = h + 2u * b;
#define XB(c) (L0 + ((c) % 5 == 0 ? 3u * PS : (c) % 5 == 1 ? VOFF : (c) % 5 == 2 ? 4u * PS : (c) % 5 == 3 ? 0u : 2u * PS))
#define WB(c) (L0 + ((c) % 5 == 0 ? 3u * PS + VOFF : (c) % 5 == 1 ? PS + VOFF : (c) % 5 == 2 ? 4u * PS + VOFF : (c) % 5 == 3 ? PS : 2u * PS + VOFF))
#define QL(c, k) do { const unsigned cc_ = ((c) + crot) & 7u; \
    if ((k) == 0) DMA1(XW_POL, srdX, vx0, 128u * cc_, XB(c) + 2048u * w); \
    else if ((k) == 1) DMA1(XW_POL, srdX, vx1, 128u * cc_, XB(c) + 2048u * w + 1024u); \
    else if ((k) == 2) DMA1(XW_POL, srdW, vw, 524288u * cc_, WB(c) + 2048u * w); \
    else DMA1(XW_POL, srdW, vw, 524288u * cc_ + 128u, WB(c) + 2048u * w + 1024u); } while (0)
#define QLOAD(c) do { QL(c, 0); QL(c, 1); QL(c, 2); QL(c, 3); } while (0)
  QLOAD(0);
  asm volatile("s_barrier" ::: "memory");
  QLOAD(1);
  u64 epoch;
  asm volatile("s_load_dwordx2 %0, %1, 0x0\n\ts_waitcnt lgkmcnt(0)" : "=s"(epoch) : "s"(ws + WS_EPOCH + 256u * b) : "memory");
  const u64 tag = epoch * 0x9E3779B97F4A7C15ull + 0xD1B54A32D192ED03ull;
  const unsigned vkA = r8 * 256u + 64u * sub + 16u * (slot ^ (r8 >> 2));
  const unsigned vkB = r8 * 256u + 64u * sub + 16u * (slot ^ (2u + (r8 >> 2)));
  const unsigned vvA = r8 * 256u + 64u * sub + 16u * slot;
  const unsigned wrow = wq4 * 4096u;
#define DA() DMA4(srdK, vkA, vkB, (g ^ 1u) * 16384u + wrow, L0 + (g ^ 1u) * PS + wrow)
#define DB() do { if (g) DMA4(srdK, vkA, vkB, 2u * 16384u + wrow, L0 + 2u * PS + wrow); else DMA4(srdV, vvA, vvA, wrow, L0 + 2u * PS + VOFF + wrow); } while (0)
#define DC() WDMA4(g, L0 + 3u * PS + g * VOFF)
  const __amdgpu_buffer_rsrc_t srdO = __builtin_amdgcn_make_buffer_rsrc((void*)(wo + 128u * im), (short)0, (int)(4096u * 1024u - 128u * im), 0x00020000);
  const unsigned vwo = r8 * 1024u + 64u * sub + 16u * slot;
#define WDMA4(n, ldsa) do { const unsigned so_ = ((8u * hg + ((im + (n)) & 7u)) * 128u + 32u * wq4) * 1024u; \
    DMA1(WO_POL, srdO, vwo, so_, (ldsa) + wrow); DMA1(WO_POL, srdO, vwo, so_ + 8192u, (ldsa) + wrow + 1024u); \
    DMA1(WO_POL, srdO, vwo, so_ + 16384u, (ldsa) + wrow + 2048u); DMA1(WO_POL, srdO, vwo, so_ + 24576u, (ldsa) + wrow + 3072u); } while (0)

  STAMP(1);
  bf16x8 qf[8];
  {
    f32x16 qa0 = {}, qa1 = {};
    const unsigned xrd0 = 4096u * wq4 + 1024u * (l32 >> 3) + 64u * (l32 & 7u) + 16u * ((0u + hi) ^ ((l32 >> 2) & 3u));
    const unsigned xrd1 = 4096u * wq4 + 1024u * (l32 >> 3) + 64u * (l32 & 7u) + 16u * ((2u + hi) ^ ((l32 >> 2) & 3u));
    const unsigned p = lane & 3u, psw = ((p & 1u) << 1) | (p >> 1);
    const unsigned wrd = 2048u * hi + 64u * ((lane >> 2) & 3u) + 32u * ((lane >> 4) & 1u) + 8u * psw + 1024u * g;
    bf16x8 fx[2][4], fa[2][4], fb[2][4];
#define QREAD1(c, s) do { \
      fx[(c) & 1][s] = lds_rd128(XB(c) + ((s & 1) ? xrd1 : xrd0) + 512u * (s >> 1)); \
      fa[(c) & 1][s] = cat8(lds_rdtr(WB(c) + wrd + 4096u * s), lds_rdtr(WB(c) + wrd + 4096u * s + 256u)); \
      fb[(c) & 1][s] = cat8(lds_rdtr(WB(c) + wrd + 4096u * s + 512u), lds_rdtr(WB(c) + wrd + 4096u * s + 768u)); SBAR(); } while (0)
#define QMM(c, s) do { qa0 = MFMA(fa[(c) & 1][s], fx[(c) & 1][s], qa0); qa1 = MFMA(fb[(c) & 1][s], fx[(c) & 1][s], qa1); SBAR(); } while (0)
#define QSUB(c, s, LD) do { QMM((c) - 1, s); if (LD) { QL((c) + 3, s); SBAR(); } QREAD1(c, s); } while (0)
#define QSTEP(c, N, LD) do { WAIT_BAR(N); QSUB(c, 0, LD); QSUB(c, 1, LD); QSUB(c, 2, LD); QSUB(c, 3, LD); } while (0)
    WAIT_BAR(4); QREAD1(0, 0); QREAD1(0, 1); QREAD1(0, 2); QREAD1(0, 3); QLOAD(2); QLOAD(3); STAMP(2);
    QSTEP(1, 8, 1);
    QSTEP(2, 8, 1);
    QSTEP(3, 8, 1); STAMP(3);
    QSTEP(4, 8, 1); DA();
    QSTEP(5, 12, 0);
    QSTEP(6, 8, 0);
    QSTEP(7, 4, 0);
    QMM(7, 0); QMM(7, 1); QMM(7, 2); QMM(7, 3); DB(); STAMP(4);
    u32x4 mine[4];
#pragma unroll
    for (int sp = 0; sp < 2; ++sp) {
#pragma unroll
      for (int e = 0; e < 4; ++e) {
        mine[sp][e] = cvtpk(qa0[8 * sp + 2 * e] * QSCALE, qa0[8 * sp + 2 * e + 1] * QSCALE);
        mine[2 + sp][e] = cvtpk(qa1[8 * sp + 2 * e] * QSCALE, qa1[8 * sp + 2 * e + 1] * QSCALE);
      }
    }
    const unsigned qx_w = L0 + VOFF + g * PS + 4096u * wq4 + 16u * lane, qx_r = L0 + VOFF + (g ^ 1u) * PS + 4096u * wq4 + 16u * lane;
#pragma unroll
    for (int i = 0; i < 4; ++i) lds_wr128(qx_w + 1024u * i, mine[i]);
    WAIT_BAR(4);
#pragma unroll
    for (int i = 0; i < 4; ++i) {
      const bf16x8 oth = lds_rd128(qx_r + 1024u * i);
      const bf16x8 own = __builtin_bit_cast(bf16x8, mine[i]);
      qf[i] = g ? oth : own; qf[4 + i] = g ? own : oth;
    }
  }
  STAMP(5); STAMPC(20);
  const __amdgpu_buffer_rsrc_t srdF = __builtin_amdgcn_make_buffer_rsrc((void*)(ws + WS_OFRAG + (size_t)(b * NH + 8u * hg) * 32768u), (short)0, 8 * 32768, 0x00020000);
  const __amdgpu_buffer_rsrc_t srdP = __builtin_amdgcn_make_buffer_rsrc((void*)(ws + WS_PART + (size_t)(b * 8u + im) * 131072u), (short)0, 131072, 0x00020000);
  const unsigned vl16 = 16u * lane;
  const unsigned sfo = (2u * wq4 + g) * 4096u, spo = (wq4 * 4u + hg) * 8192u + g * 4096u;
  const unsigned krd0 = L0 + 2048u * (l32 >> 3) + 64u * (l32 & 7u) + 16u * ((0u + hi) ^ ((l32 >> 2) & 3u));
  const unsigned krd1 = L0 + 2048u * (l32 >> 3) + 64u * (l32 & 7u) + 16u * ((2u + hi) ^ ((l32 >> 2) & 3u));
  const unsigned trl = 8u * (lane & 3u) + 64u * ((lane >> 2) & 3u) + 32u * ((lane >> 4) & 1u) + 256u * hi;
  const unsigned vrd = L0 + VOFF + trl;
  f32x16 o0 = {}, o1 = {}, o2 = {}, o3 = {}, s0, s1, negm = {};
  PIN(negm);
  float mref = 0.f, lsum = 0.f;
  float one = 1.0f; PIN(one);
  u32x4 pf[4] = {};
  bf16x8 fr[8];
#define KFR(m, sl) lds_rd128((((m) >> 1) & 1 ? krd1 : krd0) + (sl) + 8192u * ((m) & 1) + 512u * ((m) >> 2))
#define VFR(m, sl) cat8(lds_rdtr(vrd + (sl) + 4096u * ((m) >> 2) + 512u * ((m) & 3)), lds_rdtr(vrd + (sl) + 4096u * ((m) >> 2) + 512u * ((m) & 3) + 2048u))
#define PVBLOCK() do { _Pragma("unroll") for (int m = 0; m < 16; ++m) { \
      if (m + LA < 16) fr[(m + LA) & 7] = VFR(m + LA, sl); \
      const bf16x8 pb = __builtin_bit_cast(bf16x8, pf[m >> 2]); \
      if ((m & 3) == 0) o0 = MFMA(fr[m & 7], pb, o0); \
      else if ((m & 3) == 1) o1 = MFMA(fr[m & 7], pb, o1); \
      else if ((m & 3) == 2) o2 = MFMA(fr[m & 7], pb, o2); \
      else o3 = MFMA(fr[m & 7], pb, o3); \
      SBAR(); } } while (0)
  constexpr int LA = 6;
  constexpr unsigned TR0 = 8u;
  unsigned sl = g * PS;
  if (g) {
    WDMA4(0u, L0 + 3u * PS);
    BAR_ONLY();
  }
  u64 cy0 = 0, cy1 = 0, cy2 = 0, cy3 = 0, cy4 = 0, cy5 = 0, sg[5] = {};
  for (unsigned t = 0; t < 16; ++t) {
    const unsigned i = 2u * t + g;
    CYC(cy0);
    __builtin_amdgcn_s_setprio(PRIO_M);
#pragma unroll
    for (int m = 0; m < LA; ++m) fr[m] = KFR(m, sl);
    SBAR();
#pragma unroll
    for (int m = 0; m < 16; ++m) {
      if (m + LA < 16) fr[(m + LA) & 7] = KFR(m + LA, sl);
      else fr[(m + LA) & 7] = VFR(m + LA - 16, sl);
      if (m == 0) s0 = MFMA(fr[0], qf[0], negm);
      else if (m == 1) s1 = MFMA(fr[1], qf[0], negm);
      else if (m & 1) s1 = MFMA(fr[m & 7], qf[m >> 1], s1);
      else s0 = MFMA(fr[m & 7], qf[m >> 1], s0);
      SBAR();
    }
    if (t > 0) PVBLOCK();
    __builtin_amdgcn_s_setprio(PRIO_S);
    CYC(cy1);
    if (t <= TR0) { WAIT_BAR(0); } else { WAIT_BAR(1); }
    CYC(cy2);
    const unsigned pn = i + 3u;
    if (pn <= 31u) DMA4(srdK, vkA, vkB, pn * 16384u + wrow, L0 + sl + wrow);
    if (pn <= 32u) DMA4(srdV, vvA, vvA, (pn - 2u) * 16384u + wrow, L0 + sl + VOFF + wrow);
    if (pn == 32u) DMA4(srdV, vvA, vvA, 31u * 16384u + wrow, L0 + sl + wrow);
    if (pn == 33u) { WDMA4(4u, L0 + sl); WDMA4(5u, L0 + sl + VOFF); }
    if (t >= TR0 && t < 14u) {
      const unsigned idx = 8u * (t - TR0 + 2u) + w;
      DMA1(WO_POL, srdO, vwo, ((8u * hg + ((im + (idx >> 4)) & 7u)) * 128u + 8u * (idx & 15u)) * 1024u, L0 + 3u * PS + 1024u * idx);
    } else if (t == 14u) {
      asm volatile("s_mov_b32 m0, %0\n\ts_nop 0\n\tbuffer_load_dword %1, %2, %3 offen lds" :: "s"(L0 + 5u * PS), "v"(lane * 1024u), "s"(srdO), "s"(((8u * hg + ((im + 4u + (w >> 1)) & 7u)) * 128u + 64u * (w & 1u)) * 1024u) : "memory");
    }
    sl += 2u * PS; if (sl >= 3u * PS) sl -= 3u * PS;
    CYC(cy3);
    float ts;
#define EXPS() do { float a0 = 0.f, a1 = 0.f; \
      _Pragma("unroll") for (int e = 0; e < 8; ++e) { const float e0 = __builtin_amdgcn_exp2f(s0[2 * e]), e1 = __builtin_amdgcn_exp2f(s0[2 * e + 1]); a0 += e0; a1 = __builtin_fmaf(e1, one, a1); pf[e >> 2][e & 3] = cvtpk(e0, e1); } \
      _Pragma("unroll") for (int e = 0; e < 8; ++e) { const float e0 = __builtin_amdgcn_exp2f(s1[2 * e]), e1 = __builtin_amdgcn_exp2f(s1[2 * e + 1]); a0 += e0; a1 = __builtin_fmaf(e1, one, a1); pf[2 + (e >> 2)][e & 3] = cvtpk(e0, e1); } \
      ts = a0 + a1; } while (0)
    bool redo = (t == 0);
    if (!redo) { EXPS(); redo = __any(!(ts <= SUM_THR)); }
    if (__builtin_expect(redo, 0)) {
      float mx = s0[0];
#pragma unroll
      for (int e = 1; e < 16; ++e) mx = __builtin_fmaxf(mx, s0[e]);
#pragma unroll
      for (int e = 0; e < 16; ++e) mx = __builtin_fmaxf(mx, s1[e]);
      mx = swap32_max(mx);
      const float dl = (t == 0) ? mx : __builtin_fmaxf(mx, 0.f);
      const float f = (t == 0) ? 1.f : __builtin_amdgcn_exp2f(-dl);
      mref += dl; lsum *= f;
#pragma unroll
      for (int e = 0; e < 16; ++e) { s0[e] -= dl; s1[e] -= dl; o0[e] *= f; o1[e] *= f; o2[e] *= f; o3[e] *= f; negm[e] = -mref; }
      PIN(negm);
      EXPS();
    }
    lsum += ts;
    PIN(pf[0]); PIN(pf[1]); PIN(pf[2]); PIN(pf[3]); PIN(lsum);
    SBAR();
    CYC(cy4);
    BAR_ONLY();
    CYC(cy5);
    if (t >= 2 && t < 12) { sg[0] += cy1 - cy0; sg[1] += cy2 - cy1; sg[2] += cy3 - cy2; sg[3] += cy4 - cy3; sg[4] += cy5 - cy4; }
  }
  __builtin_amdgcn_s_setprio(0);
  STAMP(10); STAMPC(21);
  GAS u64* flag1 = (GAS u64*)(ws + WS_FLAG1 + (b * 4u + hg) * 128u);
  const GAS u64* hpoll = flag1 + ((im + lane) & 7u);
  u32x4 own[4], b0[4], b1[4], b2[4];
  {
    const unsigned xa = L0 + PS + 8192u * wq4 + vl16;
    f32x4 oth[8];
    if (g) {
#pragma unroll
      for (int q = 0; q < 8; ++q) oth[q] = lds_rdf4(xa + 1024u * q);
    }
    sl = 2u * PS - g * VOFF;
#pragma unroll
    for (int m = 0; m < LA; ++m) fr[m] = VFR(m, sl);
    SBAR();
    PVBLOCK();
    {
      const f32x16 sa = g ? o0 : o2, sb = g ? o1 : o3;
#pragma unroll
      for (int q = 0; q < 4; ++q) { lds_wrf4(xa + 1024u * q, quad(sa, q)); lds_wrf4(xa + 4096u + 1024u * q, quad(sb, q)); }
    }
    if (!g) { BAR_ONLY(); }
    lsum = swap32_sum(lsum);
    *(LAS f32x2*)(size_t)(L0 + 2u * PS + VOFF + 512u * w + 8u * lane) = (f32x2){mref, lsum};
    WAIT_BAR(0);
    STAMP(11);
    if (!g) {
#pragma unroll
      for (int q = 0; q < 8; ++q) oth[q] = lds_rdf4(xa + 1024u * q);
    }
    const f32x2 st = *(const LAS f32x2*)(size_t)(L0 + 2u * PS + VOFF + 512u * (w ^ 4u) + 8u * lane);
    const float mm = __builtin_fmaxf(mref, st[0]);
    const float fa = __builtin_amdgcn_exp2f(mref - mm), fb = __builtin_amdgcn_exp2f(st[0] - mm);
    const float inv = 1.0f / (fa * lsum + fb * st[1]);
    const float ca = fa * inv, cb = fb * inv;
    const f32x16 ka = g ? o2 : o0, kb = g ? o3 : o1;
#pragma unroll
    for (int d = 0; d < 2; ++d) {
#pragma unroll
      for (int sp = 0; sp < 2; ++sp) {
        const f32x4 u0 = oth[4 * d + 2 * sp], u1 = oth[4 * d + 2 * sp + 1];
        const f32x16& mo = d ? kb : ka;
        u32x4 r;
        r[0] = cvtpk(mo[8 * sp + 0] * ca + u0[0] * cb, mo[8 * sp + 1] * ca + u0[1] * cb);
        r[1] = cvtpk(mo[8 * sp + 2] * ca + u0[2] * cb, mo[8 * sp + 3] * ca + u0[3] * cb);
        r[2] = cvtpk(mo[8 * sp + 4] * ca + u1[0] * cb, mo[8 * sp + 5] * ca + u1[1] * cb);
        r[3] = cvtpk(mo[8 * sp + 6] * ca + u1[2] * cb, mo[8 * sp + 7] * ca + u1[3] * cb);
        own[2 * d + sp] = r;
      }
    }
  }
#pragma unroll
  for (int k = 0; k < 4; ++k) __builtin_amdgcn_raw_buffer_store_b128(own[k], srdF, vl16 + 1024u * k, im * 32768u + sfo, 16);
  SBAR();
  if (!g) {
    const unsigned so = ((8u * hg + ((im + 6u + (wq4 >> 1)) & 7u)) * 128u + 64u * (wq4 & 1u)) * 1024u;
#pragma unroll
    for (int q = 0; q < 8; ++q) DMA1(WO_POL, srdO, vwo, so + 8192u * q, L0 + PS + 8192u * wq4 + 1024u * q);
  }
  f32x16 c0 = {}, c1 = {};
  const unsigned ard = L0 + trl + 8192u * g;
  bf16x8 aa[8], ab[8];
  unsigned rdy = 0u, rem = 0xFEu, id0, id1, id2;
#define ARD1(A, ua_, ks) do { A[2 * (ks)] = cat8(lds_rdtr(ua_ + 2048u * (ks)), lds_rdtr(ua_ + 2048u * (ks) + 1024u)); \
      A[2 * (ks) + 1] = cat8(lds_rdtr(ua_ + 2048u * (ks) + 512u), lds_rdtr(ua_ + 2048u * (ks) + 1536u)); } while (0)
#define UA(n) (ard + (((0x32109876u >> (4u * (n))) & 15u) << 14))
#define AREAD(A, n) do { const unsigned ua_ = UA(n); ARD1(A, ua_, 0); ARD1(A, ua_, 1); ARD1(A, ua_, 2); ARD1(A, ua_, 3); SBAR(); } while (0)
#define OLOAD(B, n) do { const unsigned so_ = ((im + (n)) & 7u) * 32768u + sfo; \
    _Pragma("unroll") for (int k_ = 0; k_ < 4; ++k_) B[k_] = __builtin_amdgcn_raw_buffer_load_b128(srdF, vl16 + 1024u * k_, so_, 16); SBAR(); } while (0)
#define PROC(A, B, AN, nn) do { const unsigned un_ = UA(nn); \
    _Pragma("unroll") for (int ks = 0; ks < 4; ++ks) { \
      c0 = MFMA(A[2 * ks], __builtin_bit_cast(bf16x8, B[ks]), c0); c1 = MFMA(A[2 * ks + 1], __builtin_bit_cast(bf16x8, B[ks]), c1); SBAR(); \
      ARD1(AN, un_, ks); SBAR(); } } while (0)
#define PROCL(A, B) do { _Pragma("unroll") for (int ks = 0; ks < 4; ++ks) { \
      c0 = MFMA(A[2 * ks], __builtin_bit_cast(bf16x8, B[ks]), c0); c1 = MFMA(A[2 * ks + 1], __builtin_bit_cast(bf16x8, B[ks]), c1); } SBAR(); } while (0)
#define PICK(id, msk) do { unsigned spin_ = 0; \
    while ((rdy & rem & (msk)) == 0u && ++spin_ < SPIN_CAP) { \
      rdy = (unsigned)__ballot(__hip_atomic_load(hpoll, __ATOMIC_RELAXED, __HIP_MEMORY_SCOPE_AGENT) == tag); \
      if (rdy & rem & (msk)) break; __builtin_amdgcn_s_sleep(2); } \
    asm volatile("" ::: "memory"); \
    id = (rdy & rem & (msk)) ? __builtin_ctz(rdy & rem & (msk)) : __builtin_ctz(rem & (msk)); rem &= ~(1u << id); } while (0)
  AREAD(aa, 0u);
  PROCL(aa, own);
  if (g) asm volatile("s_waitcnt vmcnt(0)" ::: "memory"); else asm volatile("s_waitcnt vmcnt(8)" ::: "memory");
  BAR_ONLY();
  STAMP(12);
  if (tid == 0) __hip_atomic_store(flag1 + im, tag, __ATOMIC_RELAXED, __HIP_MEMORY_SCOPE_AGENT);
  PICK(id0, 0x3Eu); OLOAD(b0, id0); AREAD(ab, id0);
  STAMP(13);
  PICK(id1, 0xFEu); OLOAD(b1, id1);
  PICK(id2, 0xFEu); OLOAD(b2, id2);
  BAR_ONLY();
  PROC(ab, b0, aa, id1);
  PICK(id0, 0xFEu); OLOAD(b0, id0);
  PROC(aa, b1, ab, id2);
  PICK(id1, 0xFEu); OLOAD(b1, id1);
  PROC(ab, b2, aa, id0);
  PICK(id2, 0xFEu); OLOAD(b2, id2);
  PROC(aa, b0, ab, id1);
  PICK(id0, 0xFEu); OLOAD(b0, id0);
  PROC(ab, b1, aa, id2);
  PROC(aa, b2, ab, id0);
  PROCL(ab, b0);
  STAMP(14);
  {
    const unsigned ra = L0 + 2u * PS + g * VOFF + 4096u * wq4 + vl16, rb = L0 + 2u * PS + (g ^ 1u) * VOFF + 4096u * wq4 + vl16;
    const f32x16 snd = g ? c0 : c1;
#pragma unroll
    for (int q = 0; q < 4; ++q) lds_wrf4(ra + 1024u * q, quad(snd, q));
    BAR_ONLY();
    f32x16 kp = g ? c1 : c0;
#pragma unroll
    for (int q = 0; q < 4; ++q) { const f32x4 u = lds_rdf4(rb + 1024u * q); kp[4 * q] += u[0]; kp[4 * q + 1] += u[1]; kp[4 * q + 2] += u[2]; kp[4 * q + 3] += u[3]; }
    if (wq4 != hg) {
#pragma unroll
      for (int q = 0; q < 4; ++q) __builtin_amdgcn_raw_buffer_store_b128(__builtin_bit_cast(u32x4, quad(kp, q)), srdP, vl16 + 1024u * q, spo, 16);
    }
    WAIT_BAR(0);
    STAMP(15);
    GAS u64* flag2 = (GAS u64*)(ws + WS_FLAG2 + (b * 8u + im) * 128u);
    if (tid == 64u * ((hg + 1u) & 3u)) __hip_atomic_store(flag2 + hg, tag, __ATOMIC_RELAXED, __HIP_MEMORY_SCOPE_AGENT);
    if (wq4 == hg) {
      poll_flags(flag2 + (lane & 3u), tag, (lane & 3u) == hg);
      STAMP(16);
      u32x4 p0[4], p1[4], p2[4];
      const unsigned sin = hg * 32768u + g * 4096u;
#pragma unroll
      for (int q = 0; q < 4; ++q) {
        p0[q] = __builtin_amdgcn_raw_buffer_load_b128(srdP, vl16 + 1024u * q, sin + ((hg + 1u) & 3u) * 8192u, 16);
        p1[q] = __builtin_amdgcn_raw_buffer_load_b128(srdP, vl16 + 1024u * q, sin + ((hg + 2u) & 3u) * 8192u, 16);
        p2[q] = __builtin_amdgcn_raw_buffer_load_b128(srdP, vl16 + 1024u * q, sin + ((hg + 3u) & 3u) * 8192u, 16);
      }
      if (h == 0u && tid == 0u) *(GAS u64*)(ws + WS_EPOCH + 256u * b) = epoch + 1u;
      unsigned pk[8];
#pragma unroll
      for (int q = 0; q < 4; ++q) {
        const f32x4 a = quad(kp, q) + __builtin_bit_cast(f32x4, p0[q]) + __builtin_bit_cast(f32x4, p1[q]) + __builtin_bit_cast(f32x4, p2[q]);
        pk[2 * q] = cvtpk(a[0], a[1]); pk[2 * q + 1] = cvtpk(a[2], a[3]);
      }
      char* dst = out + ((size_t)(b * SQ + 32u * hg + l32) * DM + 64u * im + 32u * g) * 2u + 16u * hi;
#pragma unroll
      for (int q = 0; q < 4; q += 2) {
        auto r0 = __builtin_amdgcn_permlane32_swap(pk[2 * q], pk[2 * q + 2], false, false);
        auto r1 = __builtin_amdgcn_permlane32_swap(pk[2 * q + 1], pk[2 * q + 3], false, false);
        *(u32x4*)(dst + 16 * q) = (u32x4){r0[0], r1[0], r0[1], r1[1]};
      }
    }
  }
}

extern "C" void kernel_launch(void* const* d_in, const int* in_sizes, int n_in, void* d_out, int out_size, void* d_ws, size_t ws_size, hipStream_t stream) {
  for (int i = 0; i < 7; ++i) { void* p = i < 5 ? d_in[i] : i == 5 ? d_out : d_ws; hipPointerAttribute_t a; hipError_t e = hipPointerGetAttributes(&a, p); void* base = nullptr; size_t sz = 0; hipMemGetAddressRange((hipDeviceptr_t*)&base, &sz, p);
    fprintf(stderr, "ptr %d: %p err %d type %d managed %d flags %u base %p size %zu\n", i, p, (int)e, (int)a.type, a.isManaged, a.allocationFlags, base, sz); }
  mha_kernel<<<256, 512, 0, stream>>>((const char*)d_in[0], (const char*)d_in[1], (const char*)d_in[2], (const char*)d_in[3], (const char*)d_in[4], (char*)d_out, (char*)d_ws + WS_BASE);
}
```

```cpp
#include <hip/hip_runtime.h>
#include <hip/hip_bf16.h>

typedef __bf16 bf16x8 __attribute__((ext_vector_type(8)));
typedef __bf16 bf16x4 __attribute__((ext_vector_type(4)));
typedef __bf16 bf16x2 __attribute__((ext_vector_type(2)));
typedef float f32x16 __attribute__((ext_vector_type(16)));
typedef float f32x4 __attribute__((ext_vector_type(4)));
typedef float f32x2 __attribute__((ext_vector_type(2)));
typedef unsigned u32x4 __attribute__((ext_vector_type(4)));
typedef unsigned long long u64;
#define LAS __attribute__((address_space(3)))
#define GAS __attribute__((address_space(1)))
#define DI __device__ __forceinline__
#define SBAR() __builtin_amdgcn_sched_barrier(0)
#define PIN(x) asm volatile("" : "+v"(x))
#define MFMA(a, b, c) __builtin_amdgcn_mfma_f32_32x32x16_bf16((a), (b), (c), 0, 0, 0)

#define PRIO_M 0
#define PRIO_S 1
#define PRETOUCH 1
#define XW_POL ""
#define WO_POL ""
#define WS_BASE 0u
constexpr int NB = 8, SQ = 128, DM = 512, NH = 32, SK = 2048, DH = 128;
constexpr unsigned PS = 32768u;
constexpr unsigned VOFF = 16384u;
constexpr float QSCALE = 0.08838834764831845f * 1.4426950408889634f;
constexpr float SUM_THR = 65536.0f;
constexpr unsigned WS_EPOCH = 0u, WS_FLAG1 = 4096u, WS_FLAG2 = 65536u, WS_OFRAG = 1u << 20, WS_PART = 16u << 20;
constexpr unsigned SPIN_CAP = 1u << 18;

DI unsigned rfl(unsigned x) { return __builtin_amdgcn_readfirstlane(x); }
DI unsigned cvtpk(float lo, float hi) { f32x2 v = {lo, hi}; bf16x2 b = __builtin_convertvector(v, bf16x2); return __builtin_bit_cast(unsigned, b); }
DI bf16x8 lds_rd128(unsigned addr) { return *(const LAS bf16x8*)(size_t)addr; }
DI f32x4 lds_rdf4(unsigned addr) { return *(const LAS f32x4*)(size_t)addr; }
DI bf16x4 lds_rdtr(unsigned addr) { return __builtin_amdgcn_ds_read_tr16_b64_v4bf16((LAS bf16x4*)(size_t)addr); }
DI bf16x8 cat8(bf16x4 a, bf16x4 b) { return __builtin_shufflevector(a, b, 0, 1, 2, 3, 4, 5, 6, 7); }
DI void lds_wr128(unsigned addr, u32x4 v) { *(LAS u32x4*)(size_t)addr = v; }
DI void lds_wrf4(unsigned addr, f32x4 v) { *(LAS f32x4*)(size_t)addr = v; }
DI float swap32_max(float v) { auto r = __builtin_amdgcn_permlane32_swap(__float_as_uint(v), __float_as_uint(v), false, false); return __builtin_fmaxf(__uint_as_float(r[0]), __uint_as_float(r[1])); }
DI float swap32_sum(float v) { auto r = __builtin_amdgcn_permlane32_swap(__float_as_uint(v), __float_as_uint(v), false, false); return __uint_as_float(r[0]) + __uint_as_float(r[1]); }
DI f32x4 quad(const f32x16& v, int q) { return (f32x4){v[4 * q], v[4 * q + 1], v[4 * q + 2], v[4 * q + 3]}; }
DI void poll_flags(const GAS u64* p, u64 tag, bool skip) {
  for (unsigned spin = 0; spin < SPIN_CAP; ++spin) {
    const u64 v = __hip_atomic_load(p, __ATOMIC_RELAXED, __HIP_MEMORY_SCOPE_AGENT);
    if (__all(skip || v == tag)) break;
    __builtin_amdgcn_s_sleep(2);
  }
  asm volatile("" ::: "memory");
}

#define DMA4(srd, voffA, voffB, soff, m0a, par) \
  asm volatile("s_mov_b32 m0, %0\n\ts_nop 0\n\t" \
               "buffer_load_dwordx4 %2, %4, %5 offen nt lds\n\t" \
               "buffer_load_dwordx4 %3, %4, %5 offen offset:2048 nt lds\n\t" \
               "s_mov_b32 m0, %1\n\ts_nop 0\n\t" \
               "buffer_load_dwordx4 %2, %4, %6 offen nt lds\n\t" \
               "buffer_load_dwordx4 %3, %4, %6 offen offset:2048 nt lds" \
               :: "s"(m0a), "s"((m0a) + 1024u), "v"(voffA), "v"(voffB), "s"(srd), "s"((soff) + 128u * (par)), "s"((soff) + 128u - 128u * (par)) : "memory")
#define DMA2(srd, voffA, voffB, soff, m0a) \
  asm volatile("s_mov_b32 m0, %0\n\ts_nop 0\n\t" \
               "buffer_load_dwordx4 %1, %3, %4 offen nt lds\n\t" \
               "buffer_load_dwordx4 %2, %3, %4 offen offset:2048 nt lds" \
               :: "s"(m0a), "v"(voffA), "v"(voffB), "s"(srd), "s"(soff) : "memory")
#define DMA1(pol, srd, voff, soff, m0a) \
  asm volatile("s_mov_b32 m0, %0\n\ts_nop 0\n\tbuffer_load_dwordx4 %1, %2, %3 offen" pol " lds" :: "s"(m0a), "v"(voff), "s"(srd), "s"(soff) : "memory")

#define STAMP(i)
#define STAMPC(i)
#define BAR_ONLY() asm volatile("s_waitcnt lgkmcnt(0)\n\ts_barrier" ::: "memory")
#define WAIT_BAR(N) asm volatile("s_waitcnt vmcnt(" #N ") lgkmcnt(0)\n\ts_barrier" ::: "memory")

__global__ __launch_bounds__(512, 2) void mha_kernel(const char* __restrict__ x, const char* __restrict__ kc, const char* __restrict__ vc,
                                                     const char* __restrict__ wq, const char* __restrict__ wo, char* __restrict__ out, char* ws) {
  __shared__ __attribute__((aligned(1024))) char lds[163840];
  const unsigned tid = threadIdx.x, lane = tid & 63u, l32 = lane & 31u, hi = lane >> 5;
  const unsigned w = rfl(tid >> 6), wq4 = w & 3u, g = w >> 2;
  const unsigned blk = blockIdx.x, xcd = blk & 7u, j = blk >> 3;
  const unsigned b = 4u * (xcd & 1u) + (j >> 3), hg = xcd >> 1, im = j & 7u, h = 8u * hg + im;
  const unsigned bh = b * NH + h;
  const unsigned L0 = (unsigned)(size_t)lds;
  STAMP(0); STAMPC(19);

  const __amdgpu_buffer_rsrc_t srdX = __builtin_amdgcn_make_buffer_rsrc((void*)(x + (size_t)b * (SQ * DM * 2)), (short)0, SQ * DM * 2, 0x00020000);
  const __amdgpu_buffer_rsrc_t srdW = __builtin_amdgcn_make_buffer_rsrc((void*)(wq + (size_t)h * 256), (short)0, (int)(DM * 8192u - h * 256u), 0x00020000);
  const __amdgpu_buffer_rsrc_t srdK = __builtin_amdgcn_make_buffer_rsrc((void*)(kc + (size_t)bh * (SK * 256)), (short)0, SK * 256, 0x00020000);
  const __amdgpu_buffer_rsrc_t srdV = __builtin_amdgcn_make_buffer_rsrc((void*)(vc + (size_t)bh * (SK * 256)), (short)0, SK * 256, 0x00020000);

  const unsigned sub = lane >> 5, r8 = (lane >> 2) & 7u, slot = lane & 3u;
  const unsigned vx0 = (16u * w + r8) * 1024u + 64u * sub + 16u * (slot ^ (r8 >> 2));
  const unsigned vx1 = (16u * w + 8u + r8) * 1024u + 64u * sub + 16u * (slot ^ (2u + (r8 >> 2)));
  const unsigned vw = (8u * w + r8) * 8192u + 64u * sub + 16u * slot;
  const unsigned crot = h + 2u * b;
#define XB(c) (L0 + ((c) % 5 == 0 ? 3u * PS : (c) % 5 == 1 ? VOFF : (c) % 5 == 2 ? 4u * PS : (c) % 5 == 3 ? 0u : 2u * PS))
#define WB(c) (L0 + ((c) % 5 == 0 ? 3u * PS + VOFF : (c) % 5 == 1 ? PS + VOFF : (c) % 5 == 2 ? 4u * PS + VOFF : (c) % 5 == 3 ? PS : 2u * PS + VOFF))
#define QL(c, k) do { const unsigned cc_ = ((c) + crot) & 7u; \
    if ((k) == 0) DMA1(XW_POL, srdX, vx0, 128u * cc_, XB(c) + 2048u * w); \
    else if ((k) == 1) DMA1(XW_POL, srdX, vx1, 128u * cc_, XB(c) + 2048u * w + 1024u); \
    else if ((k) == 2) DMA1(XW_POL, srdW, vw, 524288u * cc_, WB(c) + 2048u * w); \
    else DMA1(XW_POL, srdW, vw, 524288u * cc_ + 128u, WB(c) + 2048u * w + 1024u); } while (0)
#define QLOAD(c) do { QL(c, 0); QL(c, 1); QL(c, 2); QL(c, 3); } while (0)
  QLOAD(0);
  asm volatile("s_barrier" ::: "memory");
  QLOAD(1);
  u64 epoch;
  asm volatile("s_load_dwordx2 %0, %1, 0x0\n\ts_waitcnt lgkmcnt(0)" : "=s"(epoch) : "s"(ws + WS_EPOCH + 256u * b) : "memory");
  const u64 tag = epoch * 0x9E3779B97F4A7C15ull + 0xD1B54A32D192ED03ull;
  const unsigned vkA = r8 * 256u + 64u * sub + 16u * (slot ^ (r8 >> 2));
  const unsigned vkB = r8 * 256u + 64u * sub + 16u * (slot ^ (2u + (r8 >> 2)));
  const unsigned vvA = r8 * 256u + 64u * sub + 16u * slot;
  const unsigned wrow = wq4 * 4096u;
#define DA() DMA4(srdK, vkA, vkB, (g ^ 1u) * 16384u + wrow, L0 + (g ^ 1u) * PS + wrow, g ^ 1u)
#define DB(hf) do { if (g) DMA2(srdK, vkA, vkB, 2u * 16384u + wrow + 128u * (hf), L0 + 2u * PS + wrow + 1024u * (hf)); else DMA2(srdV, vvA, vvA, wrow + 128u * (hf), L0 + 2u * PS + VOFF + wrow + 1024u * (hf)); } while (0)
  const __amdgpu_buffer_rsrc_t srdO = __builtin_amdgcn_make_buffer_rsrc((void*)(wo + 128u * im), (short)0, (int)(4096u * 1024u - 128u * im), 0x00020000);
  const unsigned vwo = r8 * 1024u + 64u * sub + 16u * slot;
#define WDMA4(n, ldsa) do { const unsigned so_ = ((8u * hg + ((im + (n)) & 7u)) * 128u + 32u * wq4) * 1024u; \
    DMA1(WO_POL, srdO, vwo, so_, (ldsa) + wrow); DMA1(WO_POL, srdO, vwo, so_ + 8192u, (ldsa) + wrow + 1024u); \
    DMA1(WO_POL, srdO, vwo, so_ + 16384u, (ldsa) + wrow + 2048u); DMA1(WO_POL, srdO, vwo, so_ + 24576u, (ldsa) + wrow + 3072u); } while (0)

  STAMP(1);
  bf16x8 qf[8];
  {
    f32x16 qa0 = {}, qa1 = {};
    const unsigned xrd0 = 4096u * wq4 + 1024u * (l32 >> 3) + 64u * (l32 & 7u) + 16u * ((0u + hi) ^ ((l32 >> 2) & 3u));
    const unsigned xrd1 = 4096u * wq4 + 1024u * (l32 >> 3) + 64u * (l32 & 7u) + 16u * ((2u + hi) ^ ((l32 >> 2) & 3u));
    const unsigned p = lane & 3u, psw = ((p & 1u) << 1) | (p >> 1);
    const unsigned wrd = 2048u * hi + 64u * ((lane >> 2) & 3u) + 32u * ((lane >> 4) & 1u) + 8u * psw + 1024u * g;
    bf16x8 fx[2][4], fa[2][4], fb[2][4];
#define QREAD1(c, s) do { \
      fx[(c) & 1][s] = lds_rd128(XB(c) + ((s & 1) ? xrd1 : xrd0) + 512u * (s >> 1)); \
      fa[(c) & 1][s] = cat8(lds_rdtr(WB(c) + wrd + 4096u * s), lds_rdtr(WB(c) + wrd + 4096u * s + 256u)); \
      fb[(c) & 1][s] = cat8(lds_rdtr(WB(c) + wrd + 4096u * s + 512u), lds_rdtr(WB(c) + wrd + 4096u * s + 768u)); SBAR(); } while (0)
#define QMM(c, s) do { qa0 = MFMA(fa[(c) & 1][s], fx[(c) & 1][s], qa0); qa1 = MFMA(fb[(c) & 1][s], fx[(c) & 1][s], qa1); SBAR(); } while (0)
#define QSUB(c, s, LD) do { QMM((c) - 1, s); if (LD) { QL((c) + 3, s); SBAR(); } QREAD1(c, s); } while (0)
#define QSTEP(c, N, LD) do { WAIT_BAR(N); QSUB(c, 0, LD); QSUB(c, 1, LD); QSUB(c, 2, LD); QSUB(c, 3, LD); } while (0)
    WAIT_BAR(4); QREAD1(0, 0); QREAD1(0, 1); QREAD1(0, 2); QREAD1(0, 3); QLOAD(2); QLOAD(3); STAMP(2);
    QSTEP(1, 8, 1);
    QSTEP(2, 8, 1);
    QSTEP(3, 8, 1); STAMP(3);
    QSTEP(4, 8, 1); DA();
    QSTEP(5, 12, 0); DB(0);
    QSTEP(6, 10, 0);
    QSTEP(7, 6, 0); DB(1);
    QMM(7, 0); QMM(7, 1); QMM(7, 2); QMM(7, 3); STAMP(4);
    u32x4 mine[4];
#pragma unroll
    for (int sp = 0; sp < 2; ++sp) {
#pragma unroll
      for (int e = 0; e < 4; ++e) {
        mine[sp][e] = cvtpk(qa0[8 * sp + 2 * e] * QSCALE, qa0[8 * sp + 2 * e + 1] * QSCALE);
        mine[2 + sp][e] = cvtpk(qa1[8 * sp + 2 * e] * QSCALE, qa1[8 * sp + 2 * e + 1] * QSCALE);
      }
    }
    const unsigned qx_w = L0 + VOFF + g * PS + 4096u * wq4 + 16u * lane, qx_r = L0 + VOFF + (g ^ 1u) * PS + 4096u * wq4 + 16u * lane;
#pragma unroll
    for (int i = 0; i < 4; ++i) lds_wr128(qx_w + 1024u * i, mine[i]);
    WAIT_BAR(4);
#pragma unroll
    for (int i = 0; i < 4; ++i) {
      qf[i] = __builtin_bit_cast(bf16x8, mine[i]); qf[4 + i] = lds_rd128(qx_r + 1024u * i);
    }
  }
  STAMP(5); STAMPC(20);
  const unsigned bsw = ((b & 3u) | ((b >> 2) << 4));
  const __amdgpu_buffer_rsrc_t srdF = __builtin_amdgcn_make_buffer_rsrc((void*)(ws + WS_OFRAG + (size_t)(bsw + 4u * hg) * 262144u), (short)0, 262144, 0x00020000);
  const __amdgpu_buffer_rsrc_t srdP = __builtin_amdgcn_make_buffer_rsrc((void*)(ws + WS_PART + (size_t)bsw * 262144u + im * 32768u), (short)0, 4 << 20, 0x00020000);
  const unsigned vl16 = 16u * lane;
  const unsigned sfo = (2u * wq4 + g) * 4096u, spo = hg * 1048576u + sfo;
  GAS u64* flag1 = (GAS u64*)(ws + WS_FLAG1 + (b * 4u + hg) * 1024u);
  GAS u64* flag2 = (GAS u64*)(ws + WS_FLAG2 + (b * 8u + im) * 512u);
  const unsigned krd0 = L0 + 2048u * (l32 >> 3) + 64u * (l32 & 7u) + 16u * ((0u + hi) ^ ((l32 >> 2) & 3u));
  const unsigned krd1 = L0 + 2048u * (l32 >> 3) + 64u * (l32 & 7u) + 16u * ((2u + hi) ^ ((l32 >> 2) & 3u));
  const unsigned trl = 8u * (lane & 3u) + 64u * ((lane >> 2) & 3u) + 32u * ((lane >> 4) & 1u) + 256u * hi;
  const unsigned vrd = L0 + VOFF + trl;
  f32x16 o0 = {}, o1 = {}, o2 = {}, o3 = {}, s0, s1, negm;
  float mref = 0.f, lsum = 0.f;
  float one = 1.0f; PIN(one);
  u32x4 pf[4];
  bf16x8 fr[8];
#define KFR(m, sl) lds_rd128((((m) >> 1) & 1 ? krd1 : krd0) + (sl) + 8192u * ((m) & 1) + 512u * ((m) >> 2))
#define VFR(m, sl) cat8(lds_rdtr(vrd + (sl) + 4096u * ((m) >> 2) + 512u * ((m) & 3)), lds_rdtr(vrd + (sl) + 4096u * ((m) >> 2) + 512u * ((m) & 3) + 2048u))
#define PVBLOCK() do { _Pragma("unroll") for (int m = 0; m < 16; ++m) { \
      if (m + LA < 16) fr[(m + LA) & 7] = VFR(m + LA, sl); \
      const bf16x8 pb = __builtin_bit_cast(bf16x8, pf[m >> 2]); \
      if ((m & 3) == 0) o0 = MFMA(fr[m & 7], pb, o0); \
      else if ((m & 3) == 1) o1 = MFMA(fr[m & 7], pb, o1); \
      else if ((m & 3) == 2) o2 = MFMA(fr[m & 7], pb, o2); \
      else o3 = MFMA(fr[m & 7], pb, o3); \
      SBAR(); } } while (0)
  constexpr int LA = 6;
  unsigned sl = g * PS;
  if (g) {
    if (w == 4u) {
      const char* fp = (lane & 1u) ? (const char*)(flag2 + 16u * hg) : (const char*)(flag1 + 16u * im);
      asm volatile("s_mov_b32 m0, %0\n\ts_nop 0\n\tglobal_load_lds_dword %1, off sc1" :: "s"(L0 + 4u * PS), "v"(fp) : "memory");
    }
    BAR_ONLY();
  }
#define QKBLOCK(CINIT) do { \
    _Pragma("unroll") for (int m = 0; m < LA; ++m) fr[m] = KFR(m, sl); \
    SBAR(); \
    _Pragma("unroll") for (int m = 0; m < 16; ++m) { \
      if (m + LA < 16) fr[(m + LA) & 7] = KFR(m + LA, sl); \
      else fr[(m + LA) & 7] = VFR(m + LA - 16, sl); \
      if (m == 0) s0 = MFMA(fr[0], qf[0], CINIT); \
      else if (m == 1) s1 = MFMA(fr[1], qf[0], CINIT); \
      else if (m & 1) s1 = MFMA(fr[m & 7], qf[m >> 1], s1); \
      else s0 = MFMA(fr[m & 7], qf[m >> 1], s0); \
      SBAR(); } } while (0)
#define EXPS() do { float a0 = 0.f, a1 = 0.f; \
      _Pragma("unroll") for (int e = 0; e < 8; ++e) { const float e0 = __builtin_amdgcn_exp2f(s0[2 * e]), e1 = __builtin_amdgcn_exp2f(s0[2 * e + 1]); a0 += e0; a1 = __builtin_fmaf(e1, one, a1); pf[e >> 2][e & 3] = cvtpk(e0, e1); } \
      _Pragma("unroll") for (int e = 0; e < 8; ++e) { const float e0 = __builtin_amdgcn_exp2f(s1[2 * e]), e1 = __builtin_amdgcn_exp2f(s1[2 * e + 1]); a0 += e0; a1 = __builtin_fmaf(e1, one, a1); pf[2 + (e >> 2)][e & 3] = cvtpk(e0, e1); } \
      ts = a0 + a1; } while (0)
#define ROWMAX(mx) do { mx = s0[0]; \
      _Pragma("unroll") for (int e = 1; e < 16; ++e) mx = __builtin_fmaxf(mx, s0[e]); \
      _Pragma("unroll") for (int e = 0; e < 16; ++e) mx = __builtin_fmaxf(mx, s1[e]); \
      mx = swap32_max(mx); } while (0)
#define SHIFT(dl) do { _Pragma("unroll") for (int e = 0; e < 16; e += 2) { \
      s0[e] -= dl; s0[e + 1] = __builtin_fmaf(s0[e + 1], one, -dl); s1[e] -= dl; s1[e + 1] = __builtin_fmaf(s1[e + 1], one, -dl); } } while (0)
  float ts;
  {
    const f32x16 zero = {};
    QKBLOCK(zero);
    __builtin_amdgcn_s_setprio(PRIO_S);
    if (g) { WAIT_BAR(0); } else { BAR_ONLY(); }
    DMA4(srdK, vkA, vkB, (g + 3u) * 16384u + wrow, L0 + sl + wrow, g ^ 1u);
    DMA4(srdV, vvA, vvA, (g + 1u) * 16384u + wrow, L0 + sl + VOFF + wrow, g ^ 1u);
    sl += 2u * PS; if (sl >= 3u * PS) sl -= 3u * PS;
    ROWMAX(mref);
    SHIFT(mref);
#pragma unroll
    for (int e = 0; e < 16; ++e) negm[e] = -mref;
    PIN(negm);
    EXPS();
    lsum = ts;
    PIN(pf[0]); PIN(pf[1]); PIN(pf[2]); PIN(pf[3]); PIN(lsum);
    SBAR();
    WAIT_BAR(8);
    STAMP(6);
  }
  for (unsigned t = 1; t < 16; ++t) {
    const unsigned i = 2u * t + g;
    __builtin_amdgcn_s_setprio(PRIO_M);
    QKBLOCK(negm);
    PVBLOCK();
    __builtin_amdgcn_s_setprio(PRIO_S);
    WAIT_BAR(0);
    const unsigned pn = i + 3u;
    if (pn <= 31u) DMA4(srdK, vkA, vkB, pn * 16384u + wrow, L0 + sl + wrow, g ^ 1u);
    if (pn <= 32u) DMA4(srdV, vvA, vvA, (pn - 2u) * 16384u + wrow, L0 + sl + VOFF + wrow, g ^ 1u);
    if (pn == 32u) DMA4(srdV, vvA, vvA, 31u * 16384u + wrow, L0 + sl + wrow, 1u);
    if (pn == 33u) { WDMA4(0u, L0 + 3u * PS); WDMA4(1u, L0 + 3u * PS + VOFF); }
    sl += 2u * PS; if (sl >= 3u * PS) sl -= 3u * PS;
    EXPS();
    if (__builtin_expect(__any(!(ts <= SUM_THR)), 0)) {
      float mx; ROWMAX(mx);
      const float dl = __builtin_fmaxf(mx, 0.f);
      const float f = __builtin_amdgcn_exp2f(-dl);
      mref += dl; lsum *= f;
      SHIFT(dl);
#pragma unroll
      for (int e = 0; e < 16; ++e) { o0[e] *= f; o1[e] *= f; o2[e] *= f; o3[e] *= f; negm[e] = -mref; }
      PIN(negm);
      EXPS();
    }
    lsum += ts;
    PIN(pf[0]); PIN(pf[1]); PIN(pf[2]); PIN(pf[3]); PIN(lsum);
    SBAR();
    BAR_ONLY();
  }
  __builtin_amdgcn_s_setprio(0);
  STAMP(10); STAMPC(21);
  u32x4 own[4], b0[4], b1[4], b2[4];
  const unsigned xr = g * PS + 8192u * wq4;
  {
    const unsigned xw = L0 + (g ^ 1u) * PS + 8192u * wq4 + vl16;
    unsigned vf = vrd + 2u * PS - g * VOFF; PIN(vf);
#define VFL(m) cat8(lds_rdtr(vf + 4096u * (((m) >> 1) & 3) + 512u * (((m) & 1) + ((m) < 8 ? 2 : 0))), lds_rdtr(vf + 4096u * (((m) >> 1) & 3) + 512u * (((m) & 1) + ((m) < 8 ? 2 : 0)) + 2048u))
#pragma unroll
    for (int m = 0; m < LA; ++m) fr[m] = VFL(m);
    SBAR();
#pragma unroll
    for (int m = 0; m < 16; ++m) {
      if (m + LA < 16) fr[(m + LA) & 7] = VFL(m + LA);
      const bf16x8 pb = __builtin_bit_cast(bf16x8, pf[(m >> 1) & 3]);
      if (m < 8) { if (m & 1) o3 = MFMA(fr[m & 7], pb, o3); else o2 = MFMA(fr[m & 7], pb, o2); }
      else       { if (m & 1) o1 = MFMA(fr[m & 7], pb, o1); else o0 = MFMA(fr[m & 7], pb, o0); }
      if (m >= 10 && m < 14) { lds_wrf4(xw + 1024u * (m - 10), quad(o2, m - 10)); lds_wrf4(xw + 4096u + 1024u * (m - 10), quad(o3, m - 10)); }
      SBAR();
      if (m == 9 && !g) { BAR_ONLY(); }
    }
    lsum = swap32_sum(lsum);
    *(LAS f32x2*)(size_t)(L0 + 2u * PS + VOFF + 512u * w + 8u * lane) = (f32x2){mref, lsum};
    if (!g) { WDMA4(2u, L0 + 4u * PS); WDMA4(3u, L0 + 4u * PS + VOFF); }
    WAIT_BAR(8);
    STAMP(11);
    f32x4 oth[8];
#pragma unroll
    for (int q = 0; q < 8; ++q) oth[q] = lds_rdf4(L0 + xr + vl16 + 1024u * q);
    const f32x2 st = *(const LAS f32x2*)(size_t)(L0 + 2u * PS + VOFF + 512u * (w ^ 4u) + 8u * lane);
    const float mm = __builtin_fmaxf(mref, st[0]);
    const float fa = __builtin_amdgcn_exp2f(mref - mm), fb = __builtin_amdgcn_exp2f(st[0] - mm);
    const float inv = 1.0f / (fa * lsum + fb * st[1]);
    const float ca = fa * inv, cb = fb * inv;
#pragma unroll
    for (int d = 0; d < 2; ++d) {
#pragma unroll
      for (int sp = 0; sp < 2; ++sp) {
        const f32x4 u0 = oth[4 * d + 2 * sp], u1 = oth[4 * d + 2 * sp + 1];
        const f32x16& mo = d ? o1 : o0;
        u32x4 r;
        r[0] = cvtpk(mo[8 * sp + 0] * ca + u0[0] * cb, mo[8 * sp + 1] * ca + u0[1] * cb);
        r[1] = cvtpk(mo[8 * sp + 2] * ca + u0[2] * cb, mo[8 * sp + 3] * ca + u0[3] * cb);
        r[2] = cvtpk(mo[8 * sp + 4] * ca + u1[0] * cb, mo[8 * sp + 5] * ca + u1[1] * cb);
        r[3] = cvtpk(mo[8 * sp + 6] * ca + u1[2] * cb, mo[8 * sp + 7] * ca + u1[3] * cb);
        own[2 * d + sp] = r;
        __builtin_amdgcn_raw_buffer_store_b128(r, srdF, vl16 + 1024u * (2 * d + sp), im * 32768u + sfo, 16);
      }
    }
  }
  SBAR();
  {
    const unsigned so = ((8u * hg + ((im + 4u + (w >> 1)) & 7u)) * 128u + 64u * (w & 1u)) * 1024u;
#pragma unroll
    for (int q = 0; q < 8; ++q) DMA1(WO_POL, srdO, vwo, so + 8192u * q, L0 + xr + 1024u * q);
  }
  f32x16 c0 = {}, c1 = {};
  const unsigned ard0 = L0 + trl + 8192u * g + 512u * g, ard1 = L0 + trl + 8192u * g + 512u * (g ^ 1u);
  bf16x8 aa[8], ab[8];
  unsigned rdy = 0u;
  const GAS u64* hpoll = flag1 + 16u * im + ((im + lane) & 7u);
#define UB(n) ((n) < 4 ? 3u * PS + (n) * VOFF : ((n) - 4u) * VOFF)
#define ARD1(A, u0_, u1_, ks) do { A[2 * (ks)] = cat8(lds_rdtr(u0_ + 2048u * (ks)), lds_rdtr(u0_ + 2048u * (ks) + 1024u)); \
      A[2 * (ks) + 1] = cat8(lds_rdtr(u1_ + 2048u * (ks)), lds_rdtr(u1_ + 2048u * (ks) + 1024u)); } while (0)
#define OLOAD(B, n) do { const unsigned so_ = ((im + (n)) & 7u) * 32768u + sfo; \
    _Pragma("unroll") for (int k_ = 0; k_ < 4; ++k_) B[k_] = __builtin_amdgcn_raw_buffer_load_b128(srdF, vl16 + 1024u * k_, so_, 16); SBAR(); } while (0)
#define PROC(A, B, AN, nn) do { unsigned u0_ = ard0 + UB(nn), u1_ = ard1 + UB(nn); PIN(u0_); PIN(u1_); \
    _Pragma("unroll") for (int ks = 0; ks < 4; ++ks) { \
      c0 = MFMA(A[2 * ks], __builtin_bit_cast(bf16x8, B[ks]), c0); c1 = MFMA(A[2 * ks + 1], __builtin_bit_cast(bf16x8, B[ks]), c1); SBAR(); \
      ARD1(AN, u0_, u1_, ks); SBAR(); } } while (0)
#define PROCL(A, B) do { _Pragma("unroll") for (int ks = 0; ks < 4; ++ks) { \
      c0 = MFMA(A[2 * ks], __builtin_bit_cast(bf16x8, B[ks]), c0); c1 = MFMA(A[2 * ks + 1], __builtin_bit_cast(bf16x8, B[ks]), c1); } SBAR(); } while (0)
#define NEED(n) do { unsigned spin_ = 0; \
    while (!((rdy >> (n)) & 1u) && ++spin_ < SPIN_CAP) { \
      rdy = (unsigned)__ballot(__hip_atomic_load(hpoll, __ATOMIC_RELAXED, __HIP_MEMORY_SCOPE_AGENT) == tag); \
      if ((rdy >> (n)) & 1u) break; __builtin_amdgcn_s_sleep(2); } \
    asm volatile("" ::: "memory"); } while (0)
  { unsigned u0_ = ard0 + UB(0), u1_ = ard1 + UB(0); PIN(u0_); PIN(u1_); ARD1(aa, u0_, u1_, 0); ARD1(aa, u0_, u1_, 1); ARD1(aa, u0_, u1_, 2); ARD1(aa, u0_, u1_, 3); SBAR(); }
  PROC(aa, own, ab, 1);
  WAIT_BAR(8);
  STAMP(12);
  if (tid < 8u) __hip_atomic_store(flag1 + 16u * tid + im, tag, __ATOMIC_RELAXED, __HIP_MEMORY_SCOPE_AGENT);
  NEED(1); OLOAD(b0, 1);
  STAMP(13);
  NEED(2); OLOAD(b1, 2);
  NEED(3); OLOAD(b2, 3);
  PROC(ab, b0, aa, 2);
  NEED(4); OLOAD(b0, 4);
  PROC(aa, b1, ab, 3);
  NEED(5); OLOAD(b1, 5);
  BAR_ONLY();
  PROC(ab, b2, aa, 4);
  NEED(6); OLOAD(b2, 6);
  PROC(aa, b0, ab, 5);
  NEED(7); OLOAD(b0, 7);
  PROC(ab, b1, aa, 6);
  PROC(aa, b2, ab, 7);
  PROCL(ab, b0);
  STAMP(14);
  {
    const unsigned ra = L0 + 2u * PS + g * VOFF + 4096u * wq4 + vl16, rb = L0 + 2u * PS + (g ^ 1u) * VOFF + 4096u * wq4 + vl16;
#pragma unroll
    for (int q = 0; q < 4; ++q) lds_wrf4(ra + 1024u * q, quad(c1, q));
    BAR_ONLY();
    f32x4 kp[4];
#pragma unroll
    for (int q = 0; q < 4; ++q) kp[q] = quad(c0, q) + lds_rdf4(rb + 1024u * q);
    if (wq4 != hg) {
#pragma unroll
      for (int q = 0; q < 4; ++q) __builtin_amdgcn_raw_buffer_store_b128(__builtin_bit_cast(u32x4, kp[q]), srdP, vl16 + 1024u * q, spo, 16);
    }
    WAIT_BAR(0);
    STAMP(15);
    if (w == ((hg + 1u) & 3u) && lane < 4u) __hip_atomic_store(flag2 + 16u * lane + hg, tag, __ATOMIC_RELAXED, __HIP_MEMORY_SCOPE_AGENT);
    if (wq4 == hg) {
      poll_flags(flag2 + 16u * hg + (lane & 3u), tag, (lane & 3u) == hg);
      STAMP(16);
      u32x4 p0[4], p1[4], p2[4];
#pragma unroll
      for (int q = 0; q < 4; ++q) {
        p0[q] = __builtin_amdgcn_raw_buffer_load_b128(srdP, vl16 + 1024u * q, sfo + ((hg + 1u) & 3u) * 1048576u, 16);
        p1[q] = __builtin_amdgcn_raw_buffer_load_b128(srdP, vl16 + 1024u * q, sfo + ((hg + 2u) & 3u) * 1048576u, 16);
        p2[q] = __builtin_amdgcn_raw_buffer_load_b128(srdP, vl16 + 1024u * q, sfo + ((hg + 3u) & 3u) * 1048576u, 16);
      }
      if (h == 0u && tid == 0u) *(GAS u64*)(ws + WS_EPOCH + 256u * b) = epoch + 1u;
      unsigned pk[8];
#pragma unroll
      for (int q = 0; q < 4; ++q) {
        const f32x4 a = kp[q] + __builtin_bit_cast(f32x4, p0[q]) + __builtin_bit_cast(f32x4, p1[q]) + __builtin_bit_cast(f32x4, p2[q]);
        pk[2 * q] = cvtpk(a[0], a[1]); pk[2 * q + 1] = cvtpk(a[2], a[3]);
      }
      char* dst = out + ((size_t)(b * SQ + 32u * hg + l32) * DM + 64u * im + 32u * g) * 2u + 16u * hi;
#pragma unroll
      for (int q = 0; q < 4; q += 2) {
        auto r0 = __builtin_amdgcn_permlane32_swap(pk[2 * q], pk[2 * q + 2], false, false);
        auto r1 = __builtin_amdgcn_permlane32_swap(pk[2 * q + 1], pk[2 * q + 3], false, false);
        *(u32x4*)(dst + 16 * q) = (u32x4){r0[0], r1[0], r0[1], r1[1]};
      }
    }
  }
}

extern "C" void kernel_launch(void* const* d_in, const int* in_sizes, int n_in, void* d_out, int out_size, void* d_ws, size_t ws_size, hipStream_t stream) {
  mha_kernel<<<256, 512, 0, stream>>>((const char*)d_in[0], (const char*)d_in[1], (const char*)d_in[2], (const char*)d_in[3], (const char*)d_in[4], (char*)d_out, (char*)d_ws + WS_BASE);
}
```

```cpp
#include <hip/hip_runtime.h>
#include <hip/hip_bf16.h>

typedef __bf16 bf16x8 __attribute__((ext_vector_type(8)));
typedef __bf16 bf16x4 __attribute__((ext_vector_type(4)));
typedef __bf16 bf16x2 __attribute__((ext_vector_type(2)));
typedef float f32x16 __attribute__((ext_vector_type(16)));
typedef float f32x4 __attribute__((ext_vector_type(4)));
typedef float f32x2 __attribute__((ext_vector_type(2)));
typedef unsigned u32x4 __attribute__((ext_vector_type(4)));
typedef unsigned long long u64;
#define LAS __attribute__((address_space(3)))
#define GAS __attribute__((address_space(1)))
#define DI __device__ __forceinline__
#define SBAR() __builtin_amdgcn_sched_barrier(0)
#define PIN(x) asm volatile("" : "+v"(x))
#define MFMA(a, b, c) __builtin_amdgcn_mfma_f32_32x32x16_bf16((a), (b), (c), 0, 0, 0)

#define PRIO_M 0
#define PRIO_S 1
#define PRETOUCH 1
#define EXP_OOB 0u
#define SPLIT_ISSUE 1
#define XW_POL ""
#define WO_POL ""
#define WS_BASE 0u
constexpr int NB = 8, SQ = 128, DM = 512, NH = 32, SK = 2048, DH = 128;
constexpr unsigned PS = 32768u;
constexpr unsigned VOFF = 16384u;
constexpr float QSCALE = 0.08838834764831845f * 1.4426950408889634f;
constexpr float SUM_THR = 65536.0f;
constexpr unsigned WS_EPOCH = 0u, WS_FLAG1 = 4096u, WS_FLAG2 = 65536u, WS_OFRAG = 1u << 20, WS_PART = 16u << 20;
constexpr unsigned SPIN_CAP = 1u << 18;

DI unsigned rfl(unsigned x) { return __builtin_amdgcn_readfirstlane(x); }
DI unsigned cvtpk(float lo, float hi) { f32x2 v = {lo, hi}; bf16x2 b = __builtin_convertvector(v, bf16x2); return __builtin_bit_cast(unsigned, b); }
DI bf16x8 lds_rd128(unsigned addr) { return *(const LAS bf16x8*)(size_t)addr; }
DI f32x4 lds_rdf4(unsigned addr) { return *(const LAS f32x4*)(size_t)addr; }
DI bf16x4 lds_rdtr(unsigned addr) { return __builtin_amdgcn_ds_read_tr16_b64_v4bf16((LAS bf16x4*)(size_t)addr); }
DI bf16x8 cat8(bf16x4 a, bf16x4 b) { return __builtin_shufflevector(a, b, 0, 1, 2, 3, 4, 5, 6, 7); }
DI void lds_wr128(unsigned addr, u32x4 v) { *(LAS u32x4*)(size_t)addr = v; }
DI void lds_wrf4(unsigned addr, f32x4 v) { *(LAS f32x4*)(size_t)addr = v; }
DI float swap32_max(float v) { auto r = __builtin_amdgcn_permlane32_swap(__float_as_uint(v), __float_as_uint(v), false, false); return __builtin_fmaxf(__uint_as_float(r[0]), __uint_as_float(r[1])); }
DI float swap32_sum(float v) { auto r = __builtin_amdgcn_permlane32_swap(__float_as_uint(v), __float_as_uint(v), false, false); return __uint_as_float(r[0]) + __uint_as_float(r[1]); }
DI f32x4 quad(const f32x16& v, int q) { return (f32x4){v[4 * q], v[4 * q + 1], v[4 * q + 2], v[4 * q + 3]}; }
DI void poll_flags(const GAS u64* p, u64 tag, bool skip) {
  for (unsigned spin = 0; spin < SPIN_CAP; ++spin) {
    const u64 v = __hip_atomic_load(p, __ATOMIC_RELAXED, __HIP_MEMORY_SCOPE_AGENT);
    if (__all(skip || v == tag)) break;
    __builtin_amdgcn_s_sleep(2);
  }
  asm volatile("" ::: "memory");
}

#define DMA4(srd, voffA, voffB, soff, m0a, par) \
  asm volatile("s_mov_b32 m0, %0\n\ts_nop 0\n\t" \
               "buffer_load_dwordx4 %2, %4, %5 offen nt lds\n\t" \
               "buffer_load_dwordx4 %3, %4, %5 offen offset:2048 nt lds\n\t" \
               "s_mov_b32 m0, %1\n\ts_nop 0\n\t" \
               "buffer_load_dwordx4 %2, %4, %6 offen nt lds\n\t" \
               "buffer_load_dwordx4 %3, %4, %6 offen offset:2048 nt lds" \
               :: "s"(m0a), "s"((m0a) + 1024u), "v"(voffA), "v"(voffB), "s"(srd), "s"((soff) + 128u * (par)), "s"((soff) + 128u - 128u * (par)) : "memory")
#define DMA2(srd, voffA, voffB, soff, m0a) \
  asm volatile("s_mov_b32 m0, %0\n\ts_nop 0\n\t" \
               "buffer_load_dwordx4 %1, %3, %4 offen nt lds\n\t" \
               "buffer_load_dwordx4 %2, %3, %4 offen offset:2048 nt lds" \
               :: "s"(m0a), "v"(voffA), "v"(voffB), "s"(srd), "s"(soff) : "memory")
#define DMA1(pol, srd, voff, soff, m0a) \
  asm volatile("s_mov_b32 m0, %0\n\ts_nop 0\n\tbuffer_load_dwordx4 %1, %2, %3 offen" pol " lds" :: "s"(m0a), "v"(voff), "s"(srd), "s"(soff) : "memory")

#define STAMP(i)
#define STAMPC(i)
#define BAR_ONLY() asm volatile("s_waitcnt lgkmcnt(0)\n\ts_barrier" ::: "memory")
#define WAIT_BAR(N) asm volatile("s_waitcnt vmcnt(" #N ") lgkmcnt(0)\n\ts_barrier" ::: "memory")

__global__ __launch_bounds__(512, 2) void mha_kernel(const char* __restrict__ x, const char* __restrict__ kc, const char* __restrict__ vc,
                                                     const char* __restrict__ wq, const char* __restrict__ wo, char* __restrict__ out, char* ws) {
  __shared__ __attribute__((aligned(1024))) char lds[163840];
  const unsigned tid = threadIdx.x, lane = tid & 63u, l32 = lane & 31u, hi = lane >> 5;
  const unsigned w = rfl(tid >> 6), wq4 = w & 3u, g = w >> 2;
  const unsigned blk = blockIdx.x, xcd = blk & 7u, j = blk >> 3;
  const unsigned b = 4u * (xcd & 1u) + (j >> 3), hg = xcd >> 1, im = j & 7u, h = 8u * hg + im;
  const unsigned bh = b * NH + h;
  const unsigned L0 = (unsigned)(size_t)lds;
  STAMP(0); STAMPC(19);

  const __amdgpu_buffer_rsrc_t srdX = __builtin_amdgcn_make_buffer_rsrc((void*)(x + (size_t)b * (SQ * DM * 2)), (short)0, SQ * DM * 2, 0x00020000);
  const __amdgpu_buffer_rsrc_t srdW = __builtin_amdgcn_make_buffer_rsrc((void*)(wq + (size_t)h * 256), (short)0, (int)(DM * 8192u - h * 256u), 0x00020000);
  const __amdgpu_buffer_rsrc_t srdK = __builtin_amdgcn_make_buffer_rsrc((void*)(kc + (size_t)bh * (SK * 256)), (short)0, SK * 256, 0x00020000);
  const __amdgpu_buffer_rsrc_t srdV = __builtin_amdgcn_make_buffer_rsrc((void*)(vc + (size_t)bh * (SK * 256)), (short)0, SK * 256, 0x00020000);

  const unsigned sub = lane >> 5, r8 = (lane >> 2) & 7u, slot = lane & 3u;
  const unsigned vx0 = (16u * w + r8) * 1024u + 64u * sub + 16u * (slot ^ (r8 >> 2));
  const unsigned vx1 = (16u * w + 8u + r8) * 1024u + 64u * sub + 16u * (slot ^ (2u + (r8 >> 2)));
  const unsigned vw = (8u * w + r8) * 8192u + 64u * sub + 16u * slot;
  const unsigned crot = h + 2u * b;
#define XB(c) (L0 + ((c) % 5 == 0 ? 3u * PS : (c) % 5 == 1 ? VOFF : (c) % 5 == 2 ? 4u * PS : (c) % 5 == 3 ? 0u : 2u * PS))
#define WB(c) (L0 + ((c) % 5 == 0 ? 3u * PS + VOFF : (c) % 5 == 1 ? PS + VOFF : (c) % 5 == 2 ? 4u * PS + VOFF : (c) % 5 == 3 ? PS : 2u * PS + VOFF))
#define QL(c, k) do { const unsigned cc_ = ((c) + crot) & 7u; \
    if ((k) == 0) DMA1(XW_POL, srdX, vx0, 128u * cc_, XB(c) + 2048u * w); \
    else if ((k) == 1) DMA1(XW_POL, srdX, vx1, 128u * cc_, XB(c) + 2048u * w + 1024u); \
    else if ((k) == 2) DMA1(XW_POL, srdW, vw, 524288u * cc_, WB(c) + 2048u * w); \
    else DMA1(XW_POL, srdW, vw, 524288u * cc_ + 128u, WB(c) + 2048u * w + 1024u); } while (0)
#define QLOAD(c) do { QL(c, 0); QL(c, 1); QL(c, 2); QL(c, 3); } while (0)
  QLOAD(0);
  asm volatile("s_barrier" ::: "memory");
  QLOAD(1);
  u64 epoch;
  asm volatile("s_load_dwordx2 %0, %1, 0x0\n\ts_waitcnt lgkmcnt(0)" : "=s"(epoch) : "s"(ws + WS_EPOCH + 256u * b) : "memory");
  const u64 tag = epoch * 0x9E3779B97F4A7C15ull + 0xD1B54A32D192ED03ull;
  const unsigned vkA = r8 * 256u + 64u * sub + 16u * (slot ^ (r8 >> 2));
  const unsigned vkB = r8 * 256u + 64u * sub + 16u * (slot ^ (2u + (r8 >> 2)));
  const unsigned vvA = r8 * 256u + 64u * sub + 16u * slot;
  const unsigned wrow = wq4 * 4096u;
#define DA() DMA4(srdK, vkA, vkB, (g ^ 1u) * 16384u + wrow, L0 + (g ^ 1u) * PS + wrow, g ^ 1u)
#define DB(hf) do { if (g) DMA2(srdK, vkA, vkB, 2u * 16384u + wrow + 128u * (hf), L0 + 2u * PS + wrow + 1024u * (hf)); else DMA2(srdV, vvA, vvA, wrow + 128u * (hf), L0 + 2u * PS + VOFF + wrow + 1024u * (hf)); } while (0)
  const __amdgpu_buffer_rsrc_t srdO = __builtin_amdgcn_make_buffer_rsrc((void*)(wo + 128u * im), (short)0, (int)(4096u * 1024u - 128u * im), 0x00020000);
  const unsigned vwo = r8 * 1024u + 64u * sub + 16u * slot;
#define WDMA4(n, ldsa) do { const unsigned so_ = ((8u * hg + ((im + (n)) & 7u)) * 128u + 32u * wq4) * 1024u; \
    DMA1(WO_POL, srdO, vwo, so_, (ldsa) + wrow); DMA1(WO_POL, srdO, vwo, so_ + 8192u, (ldsa) + wrow + 1024u); \
    DMA1(WO_POL, srdO, vwo, so_ + 16384u, (ldsa) + wrow + 2048u); DMA1(WO_POL, srdO, vwo, so_ + 24576u, (ldsa) + wrow + 3072u); } while (0)

  STAMP(1);
  bf16x8 qf[8];
  {
    f32x16 qa0 = {}, qa1 = {};
    const unsigned xrd0 = 4096u * wq4 + 1024u * (l32 >> 3) + 64u * (l32 & 7u) + 16u * ((0u + hi) ^ ((l32 >> 2) & 3u));
    const unsigned xrd1 = 4096u * wq4 + 1024u * (l32 >> 3) + 64u * (l32 & 7u) + 16u * ((2u + hi) ^ ((l32 >> 2) & 3u));
    const unsigned p = lane & 3u, psw = ((p & 1u) << 1) | (p >> 1);
    const unsigned wrd = 2048u * hi + 64u * ((lane >> 2) & 3u) + 32u * ((lane >> 4) & 1u) + 8u * psw + 1024u * g;
    bf16x8 fx[2][4], fa[2][4], fb[2][4];
#define QREAD1(c, s) do { \
      fx[(c) & 1][s] = lds_rd128(XB(c) + ((s & 1) ? xrd1 : xrd0) + 512u * (s >> 1)); \
      fa[(c) & 1][s] = cat8(lds_rdtr(WB(c) + wrd + 4096u * s), lds_rdtr(WB(c) + wrd + 4096u * s + 256u)); \
      fb[(c) & 1][s] = cat8(lds_rdtr(WB(c) + wrd + 4096u * s + 512u), lds_rdtr(WB(c) + wrd + 4096u * s + 768u)); SBAR(); } while (0)
#define QMM(c, s) do { qa0 = MFMA(fa[(c) & 1][s], fx[(c) & 1][s], qa0); qa1 = MFMA(fb[(c) & 1][s], fx[(c) & 1][s], qa1); SBAR(); } while (0)
#define QSUB(c, s, LD) do { QMM((c) - 1, s); if (LD) { QL((c) + 3, s); SBAR(); } QREAD1(c, s); } while (0)
#define QSTEP(c, N, LD) do { WAIT_BAR(N); QSUB(c, 0, LD); QSUB(c, 1, LD); QSUB(c, 2, LD); QSUB(c, 3, LD); } while (0)
    WAIT_BAR(4); QREAD1(0, 0); QREAD1(0, 1); QREAD1(0, 2); QREAD1(0, 3); QLOAD(2); QLOAD(3); STAMP(2);
    QSTEP(1, 8, 1);
    QSTEP(2, 8, 1);
    QSTEP(3, 8, 1); STAMP(3);
    QSTEP(4, 8, 1); DA();
    QSTEP(5, 12, 0); DB(0);
    QSTEP(6, 10, 0);
    QSTEP(7, 6, 0); DB(1);
    QMM(7, 0); QMM(7, 1); QMM(7, 2); QMM(7, 3); STAMP(4);
    u32x4 mine[4];
#pragma unroll
    for (int sp = 0; sp < 2; ++sp) {
#pragma unroll
      for (int e = 0; e < 4; ++e) {
        mine[sp][e] = cvtpk(qa0[8 * sp + 2 * e] * QSCALE, qa0[8 * sp + 2 * e + 1] * QSCALE);
        mine[2 + sp][e] = cvtpk(qa1[8 * sp + 2 * e] * QSCALE, qa1[8 * sp + 2 * e + 1] * QSCALE);
      }
    }
    const unsigned qx_w = L0 + VOFF + g * PS + 4096u * wq4 + 16u * lane, qx_r = L0 + VOFF + (g ^ 1u) * PS + 4096u * wq4 + 16u * lane;
#pragma unroll
    for (int i = 0; i < 4; ++i) lds_wr128(qx_w + 1024u * i, mine[i]);
    WAIT_BAR(4);
#pragma unroll
    for (int i = 0; i < 4; ++i) {
      qf[i] = __builtin_bit_cast(bf16x8, mine[i]); qf[4 + i] = lds_rd128(qx_r + 1024u * i);
    }
  }
  STAMP(5); STAMPC(20);
  const unsigned bsw = ((b & 3u) | ((b >> 2) << 4));
  const __amdgpu_buffer_rsrc_t srdF = __builtin_amdgcn_make_buffer_rsrc((void*)(ws + WS_OFRAG + (size_t)(bsw + 4u * hg) * 262144u), (short)0, 262144, 0x00020000);
  const __amdgpu_buffer_rsrc_t srdP = __builtin_amdgcn_make_buffer_rsrc((void*)(ws + WS_PART + (size_t)bsw * 262144u + im * 32768u), (short)0, 4 << 20, 0x00020000);
  const unsigned vl16 = 16u * lane;
  const unsigned sfo = (2u * wq4 + g) * 4096u, spo = hg * 1048576u + sfo;
  GAS u64* flag1 = (GAS u64*)(ws + WS_FLAG1 + (b * 4u + hg) * 1024u);
  GAS u64* flag2 = (GAS u64*)(ws + WS_FLAG2 + (b * 8u + im) * 512u);
  const unsigned krd0 = L0 + 2048u * (l32 >> 3) + 64u * (l32 & 7u) + 16u * ((0u + hi) ^ ((l32 >> 2) & 3u));
  const unsigned krd1 = L0 + 2048u * (l32 >> 3) + 64u * (l32 & 7u) + 16u * ((2u + hi) ^ ((l32 >> 2) & 3u));
  const unsigned trl = 8u * (lane & 3u) + 64u * ((lane >> 2) & 3u) + 32u * ((lane >> 4) & 1u) + 256u * hi;
  const unsigned vrd = L0 + VOFF + trl;
  f32x16 o0 = {}, o1 = {}, o2 = {}, o3 = {}, s0, s1, negm;
  float mref = 0.f, lsum = 0.f;
  float one = 1.0f; PIN(one);
  u32x4 pf[4];
  bf16x8 fr[8];
#define KFR(m, sl) lds_rd128((((m) >> 1) & 1 ? krd1 : krd0) + (sl) + 8192u * ((m) & 1) + 512u * ((m) >> 2))
#define VFR(m, sl) cat8(lds_rdtr(vrd + (sl) + 4096u * ((m) >> 2) + 512u * ((m) & 3)), lds_rdtr(vrd + (sl) + 4096u * ((m) >> 2) + 512u * ((m) & 3) + 2048u))
#define PVBLOCK() do { _Pragma("unroll") for (int m = 0; m < 16; ++m) { \
      if (m + LA < 16) fr[(m + LA) & 7] = VFR(m + LA, sl); \
      const bf16x8 pb = __builtin_bit_cast(bf16x8, pf[m >> 2]); \
      if ((m & 3) == 0) o0 = MFMA(fr[m & 7], pb, o0); \
      else if ((m & 3) == 1) o1 = MFMA(fr[m & 7], pb, o1); \
      else if ((m & 3) == 2) o2 = MFMA(fr[m & 7], pb, o2); \
      else o3 = MFMA(fr[m & 7], pb, o3); \
      SBAR(); } } while (0)
  constexpr int LA = 6;
  unsigned sl = g * PS;
  if (g) {
    if (w == 4u) {
      const char* fp = (lane & 1u) ? (const char*)(flag2 + 16u * hg) : (const char*)(flag1 + 16u * im);
      asm volatile("s_mov_b32 m0, %0\n\ts_nop 0\n\tglobal_load_lds_dword %1, off sc1" :: "s"(L0 + 4u * PS), "v"(fp) : "memory");
    }
    DMA4(srdK, vkA, vkB, 3u * 16384u + wrow, L0 + 3u * PS + wrow, 1u);
    DMA4(srdV, vvA, vvA, 1u * 16384u + wrow, L0 + 3u * PS + VOFF + wrow, 1u);
    BAR_ONLY();
  }
#define QKBLOCK(CINIT) do { \
    _Pragma("unroll") for (int m = 0; m < LA; ++m) fr[m] = KFR(m, sl); \
    SBAR(); \
    _Pragma("unroll") for (int m = 0; m < 16; ++m) { \
      if (m + LA < 16) fr[(m + LA) & 7] = KFR(m + LA, sl); \
      else fr[(m + LA) & 7] = VFR(m + LA - 16, sl); \
      if (m == 0) s0 = MFMA(fr[0], qf[0], CINIT); \
      else if (m == 1) s1 = MFMA(fr[1], qf[0], CINIT); \
      else if (m & 1) s1 = MFMA(fr[m & 7], qf[m >> 1], s1); \
      else s0 = MFMA(fr[m & 7], qf[m >> 1], s0); \
      SBAR(); } } while (0)
#define EXPS() do { float a0 = 0.f, a1 = 0.f; \
      _Pragma("unroll") for (int e = 0; e < 8; ++e) { const float e0 = __builtin_amdgcn_exp2f(s0[2 * e]), e1 = __builtin_amdgcn_exp2f(s0[2 * e + 1]); a0 += e0; a1 = __builtin_fmaf(e1, one, a1); pf[e >> 2][e & 3] = cvtpk(e0, e1); } \
      _Pragma("unroll") for (int e = 0; e < 8; ++e) { const float e0 = __builtin_amdgcn_exp2f(s1[2 * e]), e1 = __builtin_amdgcn_exp2f(s1[2 * e + 1]); a0 += e0; a1 = __builtin_fmaf(e1, one, a1); pf[2 + (e >> 2)][e & 3] = cvtpk(e0, e1); } \
      ts = a0 + a1; } while (0)
#define ROWMAX(mx) do { mx = s0[0]; \
      _Pragma("unroll") for (int e = 1; e < 16; ++e) mx = __builtin_fmaxf(mx, s0[e]); \
      _Pragma("unroll") for (int e = 0; e < 16; ++e) mx = __builtin_fmaxf(mx, s1[e]); \
      mx = swap32_max(mx); } while (0)
#define SHIFT(dl) do { _Pragma("unroll") for (int e = 0; e < 16; e += 2) { \
      s0[e] -= dl; s0[e + 1] = __builtin_fmaf(s0[e + 1], one, -dl); s1[e] -= dl; s1[e + 1] = __builtin_fmaf(s1[e + 1], one, -dl); } } while (0)
  float ts;
  {
    const f32x16 zero = {};
    QKBLOCK(zero);
    __builtin_amdgcn_s_setprio(PRIO_S);
    if (g) { WAIT_BAR(8); } else { BAR_ONLY(); }
    DMA4(srdK, vkA, vkB, (g + 4u) * 16384u + wrow, L0 + sl + wrow, g);
    ROWMAX(mref);
    SHIFT(mref);
#pragma unroll
    for (int e = 0; e < 16; ++e) negm[e] = -mref;
    PIN(negm);
    EXPS();
    lsum = ts;
    PIN(pf[0]); PIN(pf[1]); PIN(pf[2]); PIN(pf[3]); PIN(lsum);
    SBAR();
    DMA4(srdV, vvA, vvA, (g + 2u) * 16384u + wrow, L0 + sl + VOFF + wrow, g);
    sl ^= 2u * PS;
    WAIT_BAR(8);
    STAMP(6);
  }
  for (unsigned t = 1; t < 16; ++t) {
    const unsigned i = 2u * t + g;
    __builtin_amdgcn_s_setprio(PRIO_M);
    QKBLOCK(negm);
    PVBLOCK();
    __builtin_amdgcn_s_setprio(PRIO_S);
    BAR_ONLY();
    const unsigned pn = i + 4u;
    if (pn <= 31u) DMA4(srdK, vkA, vkB, pn * 16384u + wrow, L0 + sl + wrow, g);
    else if (pn == 32u) DMA4(srdV, vvA, vvA, 30u * 16384u + wrow, L0 + sl + VOFF + wrow, 0u);
#define ISSUE2() do { if (pn <= 31u) DMA4(srdV, vvA, vvA, (pn - 2u) * 16384u + wrow, L0 + sl + VOFF + wrow, g); \
    else if (pn == 32u) DMA4(srdV, vvA, vvA, 31u * 16384u + wrow, L0 + sl + wrow, 1u);     \
    else if (pn == 33u) { WDMA4(0u, L0 + PS); WDMA4(1u, L0 + PS + VOFF); } } while (0)
    EXPS();
    if (__builtin_expect(__any(!(ts <= SUM_THR)), 0)) {
      float mx; ROWMAX(mx);
      const float dl = __builtin_fmaxf(mx, 0.f);
      const float f = __builtin_amdgcn_exp2f(-dl);
      mref += dl; lsum *= f;
      SHIFT(dl);
#pragma unroll
      for (int e = 0; e < 16; ++e) { o0[e] *= f; o1[e] *= f; o2[e] *= f; o3[e] *= f; negm[e] = -mref; }
      PIN(negm);
      EXPS();
    }
    lsum += ts;
    PIN(pf[0]); PIN(pf[1]); PIN(pf[2]); PIN(pf[3]); PIN(lsum);
    SBAR();
    ISSUE2();
    sl ^= 2u * PS;
    if (i == 30u) { WAIT_BAR(0); } else { WAIT_BAR(8); }
  }
  __builtin_amdgcn_s_setprio(0);
  STAMP(10); STAMPC(21);
  u32x4 own[4], b0[4], b1[4], b2[4];
  const unsigned xr = (2u + g) * PS + 8192u * wq4;
  {
    const unsigned xw = L0 + (3u - g) * PS + 8192u * wq4 + vl16;
    unsigned vf = vrd - g * VOFF; PIN(vf);
#define VFL(m) cat8(lds_rdtr(vf + 4096u * (((m) >> 1) & 3) + 512u * (((m) & 1) + ((m) < 8 ? 2 : 0))), lds_rdtr(vf + 4096u * (((m) >> 1) & 3) + 512u * (((m) & 1) + ((m) < 8 ? 2 : 0)) + 2048u))
#pragma unroll
    for (int m = 0; m < LA; ++m) fr[m] = VFL(m);
    SBAR();
#pragma unroll
    for (int m = 0; m < 16; ++m) {
      if (m + LA < 16) fr[(m + LA) & 7] = VFL(m + LA);
      const bf16x8 pb = __builtin_bit_cast(bf16x8, pf[(m >> 1) & 3]);
      if (m < 8) { if (m & 1) o3 = MFMA(fr[m & 7], pb, o3); else o2 = MFMA(fr[m & 7], pb, o2); }
      else       { if (m & 1) o1 = MFMA(fr[m & 7], pb, o1); else o0 = MFMA(fr[m & 7], pb, o0); }
      if (m >= 10 && m < 14) { lds_wrf4(xw + 1024u * (m - 10), quad(o2, m - 10)); lds_wrf4(xw + 4096u + 1024u * (m - 10), quad(o3, m - 10)); }
      SBAR();
      if (m == 9 && !g) { BAR_ONLY(); }
    }
    lsum = swap32_sum(lsum);
    *(LAS f32x2*)(size_t)(L0 + VOFF + 512u * w + 8u * lane) = (f32x2){mref, lsum};
    if (g) { WAIT_BAR(0); } else { WDMA4(2u, L0 + 4u * PS); WDMA4(3u, L0 + 4u * PS + VOFF); BAR_ONLY(); }
    STAMP(11);
    f32x4 oth[8];
#pragma unroll
    for (int q = 0; q < 8; ++q) oth[q] = lds_rdf4(L0 + xr + vl16 + 1024u * q);
    const f32x2 st = *(const LAS f32x2*)(size_t)(L0 + VOFF + 512u * (w ^ 4u) + 8u * lane);
    const float mm = __builtin_fmaxf(mref, st[0]);
    const float fa = __builtin_amdgcn_exp2f(mref - mm), fb = __builtin_amdgcn_exp2f(st[0] - mm);
    const float inv = 1.0f / (fa * lsum + fb * st[1]);
    const float ca = fa * inv, cb = fb * inv;
#pragma unroll
    for (int d = 0; d < 2; ++d) {
#pragma unroll
      for (int sp = 0; sp < 2; ++sp) {
        const f32x4 u0 = oth[4 * d + 2 * sp], u1 = oth[4 * d + 2 * sp + 1];
        const f32x16& mo = d ? o1 : o0;
        u32x4 r;
        r[0] = cvtpk(mo[8 * sp + 0] * ca + u0[0] * cb, mo[8 * sp + 1] * ca + u0[1] * cb);
        r[1] = cvtpk(mo[8 * sp + 2] * ca + u0[2] * cb, mo[8 * sp + 3] * ca + u0[3] * cb);
        r[2] = cvtpk(mo[8 * sp + 4] * ca + u1[0] * cb, mo[8 * sp + 5] * ca + u1[1] * cb);
        r[3] = cvtpk(mo[8 * sp + 6] * ca + u1[2] * cb, mo[8 * sp + 7] * ca + u1[3] * cb);
        own[2 * d + sp] = r;
        __builtin_amdgcn_raw_buffer_store_b128(r, srdF, vl16 + 1024u * (2 * d + sp), im * 32768u + sfo, 16);
      }
    }
  }
  SBAR();
  {
    const unsigned so = ((8u * hg + ((im + 4u + (w >> 1)) & 7u)) * 128u + 64u * (w & 1u)) * 1024u;
#pragma unroll
    for (int q = 0; q < 8; ++q) DMA1(WO_POL, srdO, vwo + EXP_OOB, so + 8192u * q, L0 + xr + 1024u * q);
  }
  f32x16 c0 = {}, c1 = {};
  const unsigned ard0 = L0 + trl + 8192u * g + 512u * g, ard1 = L0 + trl + 8192u * g + 512u * (g ^ 1u);
  bf16x8 aa[8], ab[8];
  unsigned rdy = 0u;
  const GAS u64* hpoll = flag1 + 16u * im + ((im + lane) & 7u);
#define UB(n) ((n) < 2 ? PS + (n) * VOFF : (n) < 4 ? 4u * PS + ((n) - 2u) * VOFF : (n) * VOFF)
#define ARD1(A, u0_, u1_, ks) do { A[2 * (ks)] = cat8(lds_rdtr(u0_ + 2048u * (ks)), lds_rdtr(u0_ + 2048u * (ks) + 1024u)); \
      A[2 * (ks) + 1] = cat8(lds_rdtr(u1_ + 2048u * (ks)), lds_rdtr(u1_ + 2048u * (ks) + 1024u)); } while (0)
#define OLOAD(B, n) do { const unsigned so_ = ((im + (n)) & 7u) * 32768u + sfo; \
    _Pragma("unroll") for (int k_ = 0; k_ < 4; ++k_) B[k_] = __builtin_amdgcn_raw_buffer_load_b128(srdF, vl16 + 1024u * k_, so_, 16); SBAR(); } while (0)
#define PROC(A, B, AN, nn) do { unsigned u0_ = ard0 + UB(nn), u1_ = ard1 + UB(nn); PIN(u0_); PIN(u1_); \
    _Pragma("unroll") for (int ks = 0; ks < 4; ++ks) { \
      c0 = MFMA(A[2 * ks], __builtin_bit_cast(bf16x8, B[ks]), c0); c1 = MFMA(A[2 * ks + 1], __builtin_bit_cast(bf16x8, B[ks]), c1); SBAR(); \
      ARD1(AN, u0_, u1_, ks); SBAR(); } } while (0)
#define PROCL(A, B) do { _Pragma("unroll") for (int ks = 0; ks < 4; ++ks) { \
      c0 = MFMA(A[2 * ks], __builtin_bit_cast(bf16x8, B[ks]), c0); c1 = MFMA(A[2 * ks + 1], __builtin_bit_cast(bf16x8, B[ks]), c1); } SBAR(); } while (0)
#define NEED(n) do { unsigned spin_ = 0; \
    while (!((rdy >> (n)) & 1u) && ++spin_ < SPIN_CAP) { \
      rdy = (unsigned)__ballot(__hip_atomic_load(hpoll, __ATOMIC_RELAXED, __HIP_MEMORY_SCOPE_AGENT) == tag); \
      if ((rdy >> (n)) & 1u) break; __builtin_amdgcn_s_sleep(2); } \
    asm volatile("" ::: "memory"); } while (0)
  { unsigned u0_ = ard0 + UB(0), u1_ = ard1 + UB(0); PIN(u0_); PIN(u1_); ARD1(aa, u0_, u1_, 0); ARD1(aa, u0_, u1_, 1); ARD1(aa, u0_, u1_, 2); ARD1(aa, u0_, u1_, 3); SBAR(); }
  PROC(aa, own, ab, 1);
  WAIT_BAR(8);
  STAMP(12);
  if (tid < 8u) __hip_atomic_store(flag1 + 16u * tid + im, tag, __ATOMIC_RELAXED, __HIP_MEMORY_SCOPE_AGENT);
  NEED(1); OLOAD(b0, 1);
  STAMP(13);
  NEED(2); OLOAD(b1, 2);
  NEED(3); OLOAD(b2, 3);
  PROC(ab, b0, aa, 2);
  NEED(4); OLOAD(b0, 4);
  PROC(aa, b1, ab, 3);
  NEED(5); OLOAD(b1, 5);
  BAR_ONLY();
  PROC(ab, b2, aa, 4);
  NEED(6); OLOAD(b2, 6);
  PROC(aa, b0, ab, 5);
  NEED(7); OLOAD(b0, 7);
  PROC(ab, b1, aa, 6);
  PROC(aa, b2, ab, 7);
  PROCL(ab, b0);
  STAMP(14);
  {
    const unsigned ra = L0 + g * VOFF + 4096u * wq4 + vl16, rb = L0 + (g ^ 1u) * VOFF + 4096u * wq4 + vl16;
#pragma unroll
    for (int q = 0; q < 4; ++q) lds_wrf4(ra + 1024u * q, quad(c1, q));
    BAR_ONLY();
    f32x4 kp[4];
#pragma unroll
    for (int q = 0; q < 4; ++q) kp[q] = quad(c0, q) + lds_rdf4(rb + 1024u * q);
    if (wq4 != hg) {
#pragma unroll
      for (int q = 0; q < 4; ++q) __builtin_amdgcn_raw_buffer_store_b128(__builtin_bit_cast(u32x4, kp[q]), srdP, vl16 + 1024u * q, spo, 16);
    }
    WAIT_BAR(0);
    STAMP(15);
    if (w == ((hg + 1u) & 3u) && lane < 4u) __hip_atomic_store(flag2 + 16u * lane + hg, tag, __ATOMIC_RELAXED, __HIP_MEMORY_SCOPE_AGENT);
    if (wq4 == hg) {
      poll_flags(flag2 + 16u * hg + (lane & 3u), tag, (lane & 3u) == hg);
      STAMP(16);
      u32x4 p0[4], p1[4], p2[4];
#pragma unroll
      for (int q = 0; q < 4; ++q) {
        p0[q] = __builtin_amdgcn_raw_buffer_load_b128(srdP, vl16 + 1024u * q, sfo + ((hg + 1u) & 3u) * 1048576u, 16);
        p1[q] = __builtin_amdgcn_raw_buffer_load_b128(srdP, vl16 + 1024u * q, sfo + ((hg + 2u) & 3u) * 1048576u, 16);
        p2[q] = __builtin_amdgcn_raw_buffer_load_b128(srdP, vl16 + 1024u * q, sfo + ((hg + 3u) & 3u) * 1048576u, 16);
      }
      if (h == 0u && tid == 0u) *(GAS u64*)(ws + WS_EPOCH + 256u * b) = epoch + 1u;
      unsigned pk[8];
#pragma unroll
      for (int q = 0; q < 4; ++q) {
        const f32x4 a = kp[q] + __builtin_bit_cast(f32x4, p0[q]) + __builtin_bit_cast(f32x4, p1[q]) + __builtin_bit_cast(f32x4, p2[q]);
        pk[2 * q] = cvtpk(a[0], a[1]); pk[2 * q + 1] = cvtpk(a[2], a[3]);
      }
      char* dst = out + ((size_t)(b * SQ + 32u * hg + l32) * DM + 64u * im + 32u * g) * 2u + 16u * hi;
#pragma unroll
      for (int q = 0; q < 4; q += 2) {
        auto r0 = __builtin_amdgcn_permlane32_swap(pk[2 * q], pk[2 * q + 2], false, false);
        auto r1 = __builtin_amdgcn_permlane32_swap(pk[2 * q + 1], pk[2 * q + 3], false, false);
        *(u32x4*)(dst + 16 * q) = (u32x4){r0[0], r1[0], r0[1], r1[1]};
      }
    }
  }
}

extern "C" void kernel_launch(void* const* d_in, const int* in_sizes, int n_in, void* d_out, int out_size, void* d_ws, size_t ws_size, hipStream_t stream) {
  mha_kernel<<<256, 512, 0, stream>>>((const char*)d_in[0], (const char*)d_in[1], (const char*)d_in[2], (const char*)d_in[3], (const char*)d_in[4], (char*)d_out, (char*)d_ws + WS_BASE);
}
```

```cpp
#include <hip/hip_runtime.h>
#include <hip/hip_bf16.h>

typedef __bf16 bf16x8 __attribute__((ext_vector_type(8)));
typedef __bf16 bf16x4 __attribute__((ext_vector_type(4)));
typedef __bf16 bf16x2 __attribute__((ext_vector_type(2)));
typedef float f32x16 __attribute__((ext_vector_type(16)));
typedef float f32x4 __attribute__((ext_vector_type(4)));
typedef float f32x2 __attribute__((ext_vector_type(2)));
typedef unsigned u32x4 __attribute__((ext_vector_type(4)));
typedef unsigned long long u64;
#define LAS __attribute__((address_space(3)))
#define GAS __attribute__((address_space(1)))
#define DI __device__ __forceinline__
#define SBAR() __builtin_amdgcn_sched_barrier(0)
#define PIN(x) asm volatile("" : "+v"(x))
#define MFMA(a, b, c) __builtin_amdgcn_mfma_f32_32x32x16_bf16((a), (b), (c), 0, 0, 0)

#define PRIO_M 0
#define PRIO_S 1
#define PRETOUCH 1
#define EXP_OOB 0u
#define SPLIT_ISSUE 1
#define WO_AUX 0
#define XW_POL ""
#define WO_POL ""
#define WS_BASE 0u
constexpr int NB = 8, SQ = 128, DM = 512, NH = 32, SK = 2048, DH = 128;
constexpr unsigned PS = 32768u;
constexpr unsigned VOFF = 16384u;
constexpr float QSCALE = 0.08838834764831845f * 1.4426950408889634f;
constexpr float SUM_THR = 65536.0f;
constexpr unsigned WS_EPOCH = 0u, WS_FLAG1 = 4096u, WS_FLAG2 = 65536u, WS_OFRAG = 1u << 20, WS_PART = 16u << 20;
constexpr unsigned SPIN_CAP = 1u << 18;

DI unsigned rfl(unsigned x) { return __builtin_amdgcn_readfirstlane(x); }
DI unsigned cvtpk(float lo, float hi) { f32x2 v = {lo, hi}; bf16x2 b = __builtin_convertvector(v, bf16x2); return __builtin_bit_cast(unsigned, b); }
DI bf16x8 lds_rd128(unsigned addr) { return *(const LAS bf16x8*)(size_t)addr; }
DI f32x4 lds_rdf4(unsigned addr) { return *(const LAS f32x4*)(size_t)addr; }
DI bf16x4 lds_rdtr(unsigned addr) { return __builtin_amdgcn_ds_read_tr16_b64_v4bf16((LAS bf16x4*)(size_t)addr); }
DI bf16x8 cat8(bf16x4 a, bf16x4 b) { return __builtin_shufflevector(a, b, 0, 1, 2, 3, 4, 5, 6, 7); }
DI void lds_wr128(unsigned addr, u32x4 v) { *(LAS u32x4*)(size_t)addr = v; }
DI void lds_wrf4(unsigned addr, f32x4 v) { *(LAS f32x4*)(size_t)addr = v; }
DI float swap32_max(float v) { auto r = __builtin_amdgcn_permlane32_swap(__float_as_uint(v), __float_as_uint(v), false, false); return __builtin_fmaxf(__uint_as_float(r[0]), __uint_as_float(r[1])); }
DI float swap32_sum(float v) { auto r = __builtin_amdgcn_permlane32_swap(__float_as_uint(v), __float_as_uint(v), false, false); return __uint_as_float(r[0]) + __uint_as_float(r[1]); }
DI f32x4 quad(const f32x16& v, int q) { return (f32x4){v[4 * q], v[4 * q + 1], v[4 * q + 2], v[4 * q + 3]}; }
DI void poll_flags(const GAS u64* p, u64 tag, bool skip) {
  for (unsigned spin = 0; spin < SPIN_CAP; ++spin) {
    const u64 v = __hip_atomic_load(p, __ATOMIC_RELAXED, __HIP_MEMORY_SCOPE_AGENT);
    if (__all(skip || v == tag)) break;
    __builtin_amdgcn_s_sleep(2);
  }
  asm volatile("" ::: "memory");
}

#define DMA4(srd, voffA, voffB, soff, m0a, par) \
  asm volatile("s_mov_b32 m0, %0\n\ts_nop 0\n\t" \
               "buffer_load_dwordx4 %2, %4, %5 offen nt lds\n\t" \
               "buffer_load_dwordx4 %3, %4, %5 offen offset:2048 nt lds\n\t" \
               "s_mov_b32 m0, %1\n\ts_nop 0\n\t" \
               "buffer_load_dwordx4 %2, %4, %6 offen nt lds\n\t" \
               "buffer_load_dwordx4 %3, %4, %6 offen offset:2048 nt lds" \
               :: "s"(m0a), "s"((m0a) + 1024u), "v"(voffA), "v"(voffB), "s"(srd), "s"((soff) + 128u * (par)), "s"((soff) + 128u - 128u * (par)) : "memory")
#define DMA2(srd, voffA, voffB, soff, m0a) \
  asm volatile("s_mov_b32 m0, %0\n\ts_nop 0\n\t" \
               "buffer_load_dwordx4 %1, %3, %4 offen nt lds\n\t" \
               "buffer_load_dwordx4 %2, %3, %4 offen offset:2048 nt lds" \
               :: "s"(m0a), "v"(voffA), "v"(voffB), "s"(srd), "s"(soff) : "memory")
#define DMA1(pol, srd, voff, soff, m0a) \
  asm volatile("s_mov_b32 m0, %0\n\ts_nop 0\n\tbuffer_load_dwordx4 %1, %2, %3 offen" pol " lds" :: "s"(m0a), "v"(voff), "s"(srd), "s"(soff) : "memory")

#define STAMP(i)
#define STAMPC(i)
#define BAR_ONLY() asm volatile("s_waitcnt lgkmcnt(0)\n\ts_barrier" ::: "memory")
#define WAIT_BAR(N) asm volatile("s_waitcnt vmcnt(" #N ") lgkmcnt(0)\n\ts_barrier" ::: "memory")

__global__ __launch_bounds__(512, 2) void mha_kernel(const char* __restrict__ x, const char* __restrict__ kc, const char* __restrict__ vc,
                                                     const char* __restrict__ wq, const char* __restrict__ wo, char* __restrict__ out, char* ws) {
  __shared__ __attribute__((aligned(1024))) char lds[163840];
  const unsigned tid = threadIdx.x, lane = tid & 63u, l32 = lane & 31u, hi = lane >> 5;
  const unsigned w = rfl(tid >> 6), wq4 = w & 3u, g = w >> 2;
  const unsigned blk = blockIdx.x, xcd = blk & 7u, j = blk >> 3;
  const unsigned b = 4u * (xcd & 1u) + (j >> 3), hg = xcd >> 1, im = j & 7u, h = 8u * hg + im;
  const unsigned bh = b * NH + h;
  const unsigned L0 = (unsigned)(size_t)lds;
  STAMP(0); STAMPC(19);

  const __amdgpu_buffer_rsrc_t srdX = __builtin_amdgcn_make_buffer_rsrc((void*)(x + (size_t)b * (SQ * DM * 2)), (short)0, SQ * DM * 2, 0x00020000);
  const __amdgpu_buffer_rsrc_t srdW = __builtin_amdgcn_make_buffer_rsrc((void*)(wq + (size_t)h * 256), (short)0, (int)(DM * 8192u - h * 256u), 0x00020000);
  const __amdgpu_buffer_rsrc_t srdK = __builtin_amdgcn_make_buffer_rsrc((void*)(kc + (size_t)bh * (SK * 256)), (short)0, SK * 256, 0x00020000);
  const __amdgpu_buffer_rsrc_t srdV = __builtin_amdgcn_make_buffer_rsrc((void*)(vc + (size_t)bh * (SK * 256)), (short)0, SK * 256, 0x00020000);

  const unsigned sub = lane >> 5, r8 = (lane >> 2) & 7u, slot = lane & 3u;
  const unsigned vx0 = (16u * w + r8) * 1024u + 64u * sub + 16u * (slot ^ (r8 >> 2));
  const unsigned vx1 = (16u * w + 8u + r8) * 1024u + 64u * sub + 16u * (slot ^ (2u + (r8 >> 2)));
  const unsigned vw = (8u * w + r8) * 8192u + 64u * sub + 16u * slot;
  const unsigned crot = h + 2u * b;
#define XB(c) (L0 + ((c) % 5 == 0 ? 3u * PS : (c) % 5 == 1 ? VOFF : (c) % 5 == 2 ? 4u * PS : (c) % 5 == 3 ? 0u : 2u * PS))
#define WB(c) (L0 + ((c) % 5 == 0 ? 3u * PS + VOFF : (c) % 5 == 1 ? PS + VOFF : (c) % 5 == 2 ? 4u * PS + VOFF : (c) % 5 == 3 ? PS : 2u * PS + VOFF))
#define QL(c, k) do { const unsigned cc_ = ((c) + crot) & 7u; \
    if ((k) == 0) DMA1(XW_POL, srdX, vx0, 128u * cc_, XB(c) + 2048u * w); \
    else if ((k) == 1) DMA1(XW_POL, srdX, vx1, 128u * cc_, XB(c) + 2048u * w + 1024u); \
    else if ((k) == 2) DMA1(XW_POL, srdW, vw, 524288u * cc_, WB(c) + 2048u * w); \
    else DMA1(XW_POL, srdW, vw, 524288u * cc_ + 128u, WB(c) + 2048u * w + 1024u); } while (0)
#define QLOAD(c) do { QL(c, 0); QL(c, 1); QL(c, 2); QL(c, 3); } while (0)
  QLOAD(0);
  asm volatile("s_barrier" ::: "memory");
  QLOAD(1);
  u64 epoch;
  asm volatile("s_load_dwordx2 %0, %1, 0x0\n\ts_waitcnt lgkmcnt(0)" : "=s"(epoch) : "s"(ws + WS_EPOCH + 256u * b) : "memory");
  const u64 tag = epoch * 0x9E3779B97F4A7C15ull + 0xD1B54A32D192ED03ull;
  const unsigned vkA = r8 * 256u + 64u * sub + 16u * (slot ^ (r8 >> 2));
  const unsigned vkB = r8 * 256u + 64u * sub + 16u * (slot ^ (2u + (r8 >> 2)));
  const unsigned vvA = r8 * 256u + 64u * sub + 16u * slot;
  const unsigned wrow = wq4 * 4096u;
#define DA() DMA4(srdK, vkA, vkB, (g ^ 1u) * 16384u + wrow, L0 + (g ^ 1u) * PS + wrow, g ^ 1u)
#define DB(hf) do { if (g) DMA2(srdK, vkA, vkB, 2u * 16384u + wrow + 128u * (hf), L0 + 2u * PS + wrow + 1024u * (hf)); else DMA2(srdV, vvA, vvA, wrow + 128u * (hf), L0 + 2u * PS + VOFF + wrow + 1024u * (hf)); } while (0)
  const __amdgpu_buffer_rsrc_t srdO = __builtin_amdgcn_make_buffer_rsrc((void*)(wo + 128u * im), (short)0, (int)(4096u * 1024u - 128u * im), 0x00020000);
  const unsigned vwo = r8 * 1024u + 64u * sub + 16u * slot;
#define WDMA4(n, ldsa) do { const unsigned so_ = ((8u * hg + ((im + (n)) & 7u)) * 128u + 32u * wq4) * 1024u; \
    DMA1(WO_POL, srdO, vwo, so_, (ldsa) + wrow); DMA1(WO_POL, srdO, vwo, so_ + 8192u, (ldsa) + wrow + 1024u); \
    DMA1(WO_POL, srdO, vwo, so_ + 16384u, (ldsa) + wrow + 2048u); DMA1(WO_POL, srdO, vwo, so_ + 24576u, (ldsa) + wrow + 3072u); } while (0)

  STAMP(1);
  bf16x8 qf[8];
  {
    f32x16 qa0 = {}, qa1 = {};
    const unsigned xrd0 = 4096u * wq4 + 1024u * (l32 >> 3) + 64u * (l32 & 7u) + 16u * ((0u + hi) ^ ((l32 >> 2) & 3u));
    const unsigned xrd1 = 4096u * wq4 + 1024u * (l32 >> 3) + 64u * (l32 & 7u) + 16u * ((2u + hi) ^ ((l32 >> 2) & 3u));
    const unsigned p = lane & 3u, psw = ((p & 1u) << 1) | (p >> 1);
    const unsigned wrd = 2048u * hi + 64u * ((lane >> 2) & 3u) + 32u * ((lane >> 4) & 1u) + 8u * psw + 1024u * g;
    bf16x8 fx[2][4], fa[2][4], fb[2][4];
#define QREAD1(c, s) do { \
      fx[(c) & 1][s] = lds_rd128(XB(c) + ((s & 1) ? xrd1 : xrd0) + 512u * (s >> 1)); \
      fa[(c) & 1][s] = cat8(lds_rdtr(WB(c) + wrd + 4096u * s), lds_rdtr(WB(c) + wrd + 4096u * s + 256u)); \
      fb[(c) & 1][s] = cat8(lds_rdtr(WB(c) + wrd + 4096u * s + 512u), lds_rdtr(WB(c) + wrd + 4096u * s + 768u)); SBAR(); } while (0)
#define QMM(c, s) do { qa0 = MFMA(fa[(c) & 1][s], fx[(c) & 1][s], qa0); qa1 = MFMA(fb[(c) & 1][s], fx[(c) & 1][s], qa1); SBAR(); } while (0)
#define QSUB(c, s, LD) do { QMM((c) - 1, s); if (LD) { QL((c) + 3, s); SBAR(); } QREAD1(c, s); } while (0)
#define QSTEP(c, N, LD) do { WAIT_BAR(N); QSUB(c, 0, LD); QSUB(c, 1, LD); QSUB(c, 2, LD); QSUB(c, 3, LD); } while (0)
    WAIT_BAR(4); QREAD1(0, 0); QREAD1(0, 1); QREAD1(0, 2); QREAD1(0, 3); QLOAD(2); QLOAD(3); STAMP(2);
    QSTEP(1, 8, 1);
    QSTEP(2, 8, 1);
    QSTEP(3, 8, 1); STAMP(3);
    QSTEP(4, 8, 1); DA();
    QSTEP(5, 12, 0); DB(0);
    QSTEP(6, 10, 0);
    QSTEP(7, 6, 0); DB(1);
    QMM(7, 0); QMM(7, 1); QMM(7, 2); QMM(7, 3); STAMP(4);
    u32x4 mine[4];
#pragma unroll
    for (int sp = 0; sp < 2; ++sp) {
#pragma unroll
      for (int e = 0; e < 4; ++e) {
        mine[sp][e] = cvtpk(qa0[8 * sp + 2 * e] * QSCALE, qa0[8 * sp + 2 * e + 1] * QSCALE);
        mine[2 + sp][e] = cvtpk(qa1[8 * sp + 2 * e] * QSCALE, qa1[8 * sp + 2 * e + 1] * QSCALE);
      }
    }
    const unsigned qx_w = L0 + VOFF + g * PS + 4096u * wq4 + 16u * lane, qx_r = L0 + VOFF + (g ^ 1u) * PS + 4096u * wq4 + 16u * lane;
#pragma unroll
    for (int i = 0; i < 4; ++i) lds_wr128(qx_w + 1024u * i, mine[i]);
    WAIT_BAR(4);
#pragma unroll
    for (int i = 0; i < 4; ++i) {
      qf[i] = __builtin_bit_cast(bf16x8, mine[i]); qf[4 + i] = lds_rd128(qx_r + 1024u * i);
    }
  }
  STAMP(5); STAMPC(20);
  const unsigned bsw = ((b & 3u) | ((b >> 2) << 4));
  const __amdgpu_buffer_rsrc_t srdF = __builtin_amdgcn_make_buffer_rsrc((void*)(ws + WS_OFRAG + (size_t)(bsw + 4u * hg) * 262144u), (short)0, 262144, 0x00020000);
  const __amdgpu_buffer_rsrc_t srdP = __builtin_amdgcn_make_buffer_rsrc((void*)(ws + WS_PART + (size_t)bsw * 262144u + im * 32768u), (short)0, 4 << 20, 0x00020000);
  const unsigned vl16 = 16u * lane;
  const unsigned sfo = (2u * wq4 + g) * 4096u, spo = hg * 1048576u + sfo;
  GAS u64* flag1 = (GAS u64*)(ws + WS_FLAG1 + (b * 4u + hg) * 1024u);
  GAS u64* flag2 = (GAS u64*)(ws + WS_FLAG2 + (b * 8u + im) * 512u);
  const unsigned krd0 = L0 + 2048u * (l32 >> 3) + 64u * (l32 & 7u) + 16u * ((0u + hi) ^ ((l32 >> 2) & 3u));
  const unsigned krd1 = L0 + 2048u * (l32 >> 3) + 64u * (l32 & 7u) + 16u * ((2u + hi) ^ ((l32 >> 2) & 3u));
  const unsigned trl = 8u * (lane & 3u) + 64u * ((lane >> 2) & 3u) + 32u * ((lane >> 4) & 1u) + 256u * hi;
  const unsigned vrd = L0 + VOFF + trl;
  f32x16 o0 = {}, o1 = {}, o2 = {}, o3 = {}, s0, s1, negm;
  float mref = 0.f, lsum = 0.f;
  float one = 1.0f; PIN(one);
  u32x4 pf[4];
  bf16x8 fr[8];
#define KFR(m, sl) lds_rd128((((m) >> 1) & 1 ? krd1 : krd0) + (sl) + 8192u * ((m) & 1) + 512u * ((m) >> 2))
#define VFR(m, sl) cat8(lds_rdtr(vrd + (sl) + 4096u * ((m) >> 2) + 512u * ((m) & 3)), lds_rdtr(vrd + (sl) + 4096u * ((m) >> 2) + 512u * ((m) & 3) + 2048u))
#define PVBLOCK() do { _Pragma("unroll") for (int m = 0; m < 16; ++m) { \
      if (m + LA < 16) fr[(m + LA) & 7] = VFR(m + LA, sl); \
      const bf16x8 pb = __builtin_bit_cast(bf16x8, pf[m >> 2]); \
      if ((m & 3) == 0) o0 = MFMA(fr[m & 7], pb, o0); \
      else if ((m & 3) == 1) o1 = MFMA(fr[m & 7], pb, o1); \
      else if ((m & 3) == 2) o2 = MFMA(fr[m & 7], pb, o2); \
      else o3 = MFMA(fr[m & 7], pb, o3); \
      SBAR(); } } while (0)
  constexpr int LA = 6;
  unsigned sl = g * PS;
  if (g) {
    if (w == 4u) {
      const char* fp = (lane & 1u) ? (const char*)(flag2 + 16u * hg) : (const char*)(flag1 + 16u * im);
      asm volatile("s_mov_b32 m0, %0\n\ts_nop 0\n\tglobal_load_lds_dword %1, off sc1" :: "s"(L0 + 4u * PS), "v"(fp) : "memory");
    }
    DMA4(srdK, vkA, vkB, 3u * 16384u + wrow, L0 + 3u * PS + wrow, 1u);
    DMA4(srdV, vvA, vvA, 1u * 16384u + wrow, L0 + 3u * PS + VOFF + wrow, 1u);
    BAR_ONLY();
  }
#define QKBLOCK(CINIT) do { \
    _Pragma("unroll") for (int m = 0; m < LA; ++m) fr[m] = KFR(m, sl); \
    SBAR(); \
    _Pragma("unroll") for (int m = 0; m < 16; ++m) { \
      if (m + LA < 16) fr[(m + LA) & 7] = KFR(m + LA, sl); \
      else fr[(m + LA) & 7] = VFR(m + LA - 16, sl); \
      if (m == 0) s0 = MFMA(fr[0], qf[0], CINIT); \
      else if (m == 1) s1 = MFMA(fr[1], qf[0], CINIT); \
      else if (m & 1) s1 = MFMA(fr[m & 7], qf[m >> 1], s1); \
      else s0 = MFMA(fr[m & 7], qf[m >> 1], s0); \
      SBAR(); } } while (0)
#define EXPS() do { float a0 = 0.f, a1 = 0.f; \
      _Pragma("unroll") for (int e = 0; e < 8; ++e) { const float e0 = __builtin_amdgcn_exp2f(s0[2 * e]), e1 = __builtin_amdgcn_exp2f(s0[2 * e + 1]); a0 += e0; a1 = __builtin_fmaf(e1, one, a1); pf[e >> 2][e & 3] = cvtpk(e0, e1); } \
      _Pragma("unroll") for (int e = 0; e < 8; ++e) { const float e0 = __builtin_amdgcn_exp2f(s1[2 * e]), e1 = __builtin_amdgcn_exp2f(s1[2 * e + 1]); a0 += e0; a1 = __builtin_fmaf(e1, one, a1); pf[2 + (e >> 2)][e & 3] = cvtpk(e0, e1); } \
      ts = a0 + a1; } while (0)
#define ROWMAX(mx) do { mx = s0[0]; \
      _Pragma("unroll") for (int e = 1; e < 16; ++e) mx = __builtin_fmaxf(mx, s0[e]); \
      _Pragma("unroll") for (int e = 0; e < 16; ++e) mx = __builtin_fmaxf(mx, s1[e]); \
      mx = swap32_max(mx); } while (0)
#define SHIFT(dl) do { _Pragma("unroll") for (int e = 0; e < 16; e += 2) { \
      s0[e] -= dl; s0[e + 1] = __builtin_fmaf(s0[e + 1], one, -dl); s1[e] -= dl; s1[e + 1] = __builtin_fmaf(s1[e + 1], one, -dl); } } while (0)
  float ts;
  {
    const f32x16 zero = {};
    QKBLOCK(zero);
    __builtin_amdgcn_s_setprio(PRIO_S);
    if (g) { WAIT_BAR(8); } else { BAR_ONLY(); }
    DMA4(srdK, vkA, vkB, (g + 4u) * 16384u + wrow, L0 + sl + wrow, g);
    ROWMAX(mref);
    SHIFT(mref);
#pragma unroll
    for (int e = 0; e < 16; ++e) negm[e] = -mref;
    PIN(negm);
    EXPS();
    lsum = ts;
    PIN(pf[0]); PIN(pf[1]); PIN(pf[2]); PIN(pf[3]); PIN(lsum);
    SBAR();
    DMA4(srdV, vvA, vvA, (g + 2u) * 16384u + wrow, L0 + sl + VOFF + wrow, g);
    sl ^= 2u * PS;
    WAIT_BAR(8);
    STAMP(6);
  }
  for (unsigned t = 1; t < 16; ++t) {
    const unsigned i = 2u * t + g;
    __builtin_amdgcn_s_setprio(PRIO_M);
    QKBLOCK(negm);
    PVBLOCK();
    __builtin_amdgcn_s_setprio(PRIO_S);
    BAR_ONLY();
    const unsigned pn = i + 4u;
    if (pn <= 31u) DMA4(srdK, vkA, vkB, pn * 16384u + wrow, L0 + sl + wrow, g);
    else if (pn == 32u) DMA4(srdV, vvA, vvA, 30u * 16384u + wrow, L0 + sl + VOFF + wrow, 0u);
#define ISSUE2() do { if (pn <= 31u) DMA4(srdV, vvA, vvA, (pn - 2u) * 16384u + wrow, L0 + sl + VOFF + wrow, g); \
    else if (pn == 32u) DMA4(srdV, vvA, vvA, 31u * 16384u + wrow, L0 + sl + wrow, 1u);     \
    else if (pn == 33u) { WDMA4(0u, L0 + PS); WDMA4(1u, L0 + PS + VOFF); } } while (0)
    EXPS();
    if (__builtin_expect(__any(!(ts <= SUM_THR)), 0)) {
      float mx; ROWMAX(mx);
      const float dl = __builtin_fmaxf(mx, 0.f);
      const float f = __builtin_amdgcn_exp2f(-dl);
      mref += dl; lsum *= f;
      SHIFT(dl);
#pragma unroll
      for (int e = 0; e < 16; ++e) { o0[e] *= f; o1[e] *= f; o2[e] *= f; o3[e] *= f; negm[e] = -mref; }
      PIN(negm);
      EXPS();
    }
    lsum += ts;
    PIN(pf[0]); PIN(pf[1]); PIN(pf[2]); PIN(pf[3]); PIN(lsum);
    SBAR();
    ISSUE2();
    sl ^= 2u * PS;
    if (i == 30u) { WAIT_BAR(0); } else { WAIT_BAR(8); }
  }
  __builtin_amdgcn_s_setprio(0);
  STAMP(10); STAMPC(21);
  u32x4 wreg[8];
  u32x4 own[4], b0[4], b1[4], b2[4];
  const unsigned xr = (2u + g) * PS + 8192u * wq4;
  {
    const unsigned xw = L0 + (3u - g) * PS + 8192u * wq4 + vl16;
    unsigned vf = vrd - g * VOFF; PIN(vf);
#define VFL(m) cat8(lds_rdtr(vf + 4096u * (((m) >> 1) & 3) + 512u * (((m) & 1) + ((m) < 8 ? 2 : 0))), lds_rdtr(vf + 4096u * (((m) >> 1) & 3) + 512u * (((m) & 1) + ((m) < 8 ? 2 : 0)) + 2048u))
#pragma unroll
    for (int m = 0; m < LA; ++m) fr[m] = VFL(m);
    SBAR();
    const unsigned swo = ((8u * hg + ((im + 4u + (w >> 1)) & 7u)) * 128u + 64u * (w & 1u)) * 1024u;
#pragma unroll
    for (int m = 0; m < 16; ++m) {
      if (m + LA < 16) fr[(m + LA) & 7] = VFL(m + LA);
      if (m < 8) wreg[m] = __builtin_amdgcn_raw_buffer_load_b128(srdO, vwo, swo + 8192u * m, WO_AUX);
      const bf16x8 pb = __builtin_bit_cast(bf16x8, pf[(m >> 1) & 3]);
      if (m < 8) { if (m & 1) o3 = MFMA(fr[m & 7], pb, o3); else o2 = MFMA(fr[m & 7], pb, o2); }
      else       { if (m & 1) o1 = MFMA(fr[m & 7], pb, o1); else o0 = MFMA(fr[m & 7], pb, o0); }
      if (m >= 10 && m < 14) { lds_wrf4(xw + 1024u * (m - 10), quad(o2, m - 10)); lds_wrf4(xw + 4096u + 1024u * (m - 10), quad(o3, m - 10)); }
      SBAR();
      if (m == 9 && !g) { BAR_ONLY(); }
    }
    lsum = swap32_sum(lsum);
    *(LAS f32x2*)(size_t)(L0 + VOFF + 512u * w + 8u * lane) = (f32x2){mref, lsum};
    if (g) { WAIT_BAR(8); } else { WDMA4(2u, L0 + 4u * PS); WDMA4(3u, L0 + 4u * PS + VOFF); BAR_ONLY(); }
    STAMP(11);
    f32x4 oth[8];
#pragma unroll
    for (int q = 0; q < 8; ++q) oth[q] = lds_rdf4(L0 + xr + vl16 + 1024u * q);
    const f32x2 st = *(const LAS f32x2*)(size_t)(L0 + VOFF + 512u * (w ^ 4u) + 8u * lane);
    const float mm = __builtin_fmaxf(mref, st[0]);
    const float fa = __builtin_amdgcn_exp2f(mref - mm), fb = __builtin_amdgcn_exp2f(st[0] - mm);
    const float inv = 1.0f / (fa * lsum + fb * st[1]);
    const float ca = fa * inv, cb = fb * inv;
#pragma unroll
    for (int d = 0; d < 2; ++d) {
#pragma unroll
      for (int sp = 0; sp < 2; ++sp) {
        const f32x4 u0 = oth[4 * d + 2 * sp], u1 = oth[4 * d + 2 * sp + 1];
        const f32x16& mo = d ? o1 : o0;
        u32x4 r;
        r[0] = cvtpk(mo[8 * sp + 0] * ca + u0[0] * cb, mo[8 * sp + 1] * ca + u0[1] * cb);
        r[1] = cvtpk(mo[8 * sp + 2] * ca + u0[2] * cb, mo[8 * sp + 3] * ca + u0[3] * cb);
        r[2] = cvtpk(mo[8 * sp + 4] * ca + u1[0] * cb, mo[8 * sp + 5] * ca + u1[1] * cb);
        r[3] = cvtpk(mo[8 * sp + 6] * ca + u1[2] * cb, mo[8 * sp + 7] * ca + u1[3] * cb);
        own[2 * d + sp] = r;
        __builtin_amdgcn_raw_buffer_store_b128(r, srdF, vl16 + 1024u * (2 * d + sp), im * 32768u + sfo, 16);
      }
    }
  }
  SBAR();
  f32x16 c0 = {}, c1 = {};
  const unsigned ard0 = L0 + trl + 8192u * g + 512u * g, ard1 = L0 + trl + 8192u * g + 512u * (g ^ 1u);
  bf16x8 aa[8], ab[8];
  unsigned rdy = 0u;
  const GAS u64* hpoll = flag1 + 16u * im + ((im + lane) & 7u);
#define UB(n) ((n) < 2 ? PS + (n) * VOFF : (n) < 4 ? 4u * PS + ((n) - 2u) * VOFF : (n) * VOFF)
#define ARD1(A, u0_, u1_, ks) do { A[2 * (ks)] = cat8(lds_rdtr(u0_ + 2048u * (ks)), lds_rdtr(u0_ + 2048u * (ks) + 1024u)); \
      A[2 * (ks) + 1] = cat8(lds_rdtr(u1_ + 2048u * (ks)), lds_rdtr(u1_ + 2048u * (ks) + 1024u)); } while (0)
#define OLOAD(B, n) do { const unsigned so_ = ((im + (n)) & 7u) * 32768u + sfo; \
    _Pragma("unroll") for (int k_ = 0; k_ < 4; ++k_) B[k_] = __builtin_amdgcn_raw_buffer_load_b128(srdF, vl16 + 1024u * k_, so_, 16); SBAR(); } while (0)
#define PROC(A, B, AN, nn) do { unsigned u0_ = ard0 + UB(nn), u1_ = ard1 + UB(nn); PIN(u0_); PIN(u1_); \
    _Pragma("unroll") for (int ks = 0; ks < 4; ++ks) { \
      c0 = MFMA(A[2 * ks], __builtin_bit_cast(bf16x8, B[ks]), c0); c1 = MFMA(A[2 * ks + 1], __builtin_bit_cast(bf16x8, B[ks]), c1); SBAR(); \
      ARD1(AN, u0_, u1_, ks); SBAR(); } } while (0)
#define PROCL(A, B) do { _Pragma("unroll") for (int ks = 0; ks < 4; ++ks) { \
      c0 = MFMA(A[2 * ks], __builtin_bit_cast(bf16x8, B[ks]), c0); c1 = MFMA(A[2 * ks + 1], __builtin_bit_cast(bf16x8, B[ks]), c1); } SBAR(); } while (0)
#define NEED(n) do { unsigned spin_ = 0; \
    while (!((rdy >> (n)) & 1u) && ++spin_ < SPIN_CAP) { \
      rdy = (unsigned)__ballot(__hip_atomic_load(hpoll, __ATOMIC_RELAXED, __HIP_MEMORY_SCOPE_AGENT) == tag); \
      if ((rdy >> (n)) & 1u) break; __builtin_amdgcn_s_sleep(2); } \
    asm volatile("" ::: "memory"); } while (0)
  { unsigned u0_ = ard0 + UB(0), u1_ = ard1 + UB(0); PIN(u0_); PIN(u1_); ARD1(aa, u0_, u1_, 0); ARD1(aa, u0_, u1_, 1); ARD1(aa, u0_, u1_, 2); ARD1(aa, u0_, u1_, 3); SBAR(); }
  PROC(aa, own, ab, 1);
#pragma unroll
  for (int q = 0; q < 8; ++q) lds_wr128(L0 + xr + vl16 + 1024u * q, wreg[q]);
  WAIT_BAR(0);
  STAMP(12);
  if (tid < 8u) __hip_atomic_store(flag1 + 16u * tid + im, tag, __ATOMIC_RELAXED, __HIP_MEMORY_SCOPE_AGENT);
  NEED(1); OLOAD(b0, 1);
  STAMP(13);
  NEED(2); OLOAD(b1, 2);
  NEED(3); OLOAD(b2, 3);
  PROC(ab, b0, aa, 2);
  NEED(4); OLOAD(b0, 4);
  PROC(aa, b1, ab, 3);
  NEED(5); OLOAD(b1, 5);
  PROC(ab, b2, aa, 4);
  NEED(6); OLOAD(b2, 6);
  PROC(aa, b0, ab, 5);
  NEED(7); OLOAD(b0, 7);
  PROC(ab, b1, aa, 6);
  PROC(aa, b2, ab, 7);
  PROCL(ab, b0);
  STAMP(14);
  {
    const unsigned ra = L0 + g * VOFF + 4096u * wq4 + vl16, rb = L0 + (g ^ 1u) * VOFF + 4096u * wq4 + vl16;
#pragma unroll
    for (int q = 0; q < 4; ++q) lds_wrf4(ra + 1024u * q, quad(c1, q));
    BAR_ONLY();
    f32x4 kp[4];
#pragma unroll
    for (int q = 0; q < 4; ++q) kp[q] = quad(c0, q) + lds_rdf4(rb + 1024u * q);
    if (wq4 != hg) {
#pragma unroll
      for (int q = 0; q < 4; ++q) __builtin_amdgcn_raw_buffer_store_b128(__builtin_bit_cast(u32x4, kp[q]), srdP, vl16 + 1024u * q, spo, 16);
    }
    WAIT_BAR(0);
    STAMP(15);
    if (w == ((hg + 1u) & 3u) && lane < 4u) __hip_atomic_store(flag2 + 16u * lane + hg, tag, __ATOMIC_RELAXED, __HIP_MEMORY_SCOPE_AGENT);
    if (wq4 == hg) {
      poll_flags(flag2 + 16u * hg + (lane & 3u), tag, (lane & 3u) == hg);
      STAMP(16);
      u32x4 p0[4], p1[4], p2[4];
#pragma unroll
      for (int q = 0; q < 4; ++q) {
        p0[q] = __builtin_amdgcn_raw_buffer_load_b128(srdP, vl16 + 1024u * q, sfo + ((hg + 1u) & 3u) * 1048576u, 16);
        p1[q] = __builtin_amdgcn_raw_buffer_load_b128(srdP, vl16 + 1024u * q, sfo + ((hg + 2u) & 3u) * 1048576u, 16);
        p2[q] = __builtin_amdgcn_raw_buffer_load_b128(srdP, vl16 + 1024u * q, sfo + ((hg + 3u) & 3u) * 1048576u, 16);
      }
      if (h == 0u && tid == 0u) *(GAS u64*)(ws + WS_EPOCH + 256u * b) = epoch + 1u;
      unsigned pk[8];
#pragma unroll
      for (int q = 0; q < 4; ++q) {
        const f32x4 a = kp[q] + __builtin_bit_cast(f32x4, p0[q]) + __builtin_bit_cast(f32x4, p1[q]) + __builtin_bit_cast(f32x4, p2[q]);
        pk[2 * q] = cvtpk(a[0], a[1]); pk[2 * q + 1] = cvtpk(a[2], a[3]);
      }
      char* dst = out + ((size_t)(b * SQ + 32u * hg + l32) * DM + 64u * im + 32u * g) * 2u + 16u * hi;
#pragma unroll
      for (int q = 0; q < 4; q += 2) {
        auto r0 = __builtin_amdgcn_permlane32_swap(pk[2 * q], pk[2 * q + 2], false, false);
        auto r1 = __builtin_amdgcn_permlane32_swap(pk[2 * q + 1], pk[2 * q + 3], false, false);
        *(u32x4*)(dst + 16 * q) = (u32x4){r0[0], r1[0], r0[1], r1[1]};
      }
    }
  }
}

extern "C" void kernel_launch(void* const* d_in, const int* in_sizes, int n_in, void* d_out, int out_size, void* d_ws, size_t ws_size, hipStream_t stream) {
  mha_kernel<<<256, 512, 0, stream>>>((const char*)d_in[0], (const char*)d_in[1], (const char*)d_in[2], (const char*)d_in[3], (const char*)d_in[4], (char*)d_out, (char*)d_ws + WS_BASE);
}
```

```cpp
#include <hip/hip_runtime.h>
#include <hip/hip_bf16.h>

typedef __bf16 bf16x8 __attribute__((ext_vector_type(8)));
typedef __bf16 bf16x4 __attribute__((ext_vector_type(4)));
typedef __bf16 bf16x2 __attribute__((ext_vector_type(2)));
typedef float f32x16 __attribute__((ext_vector_type(16)));
typedef float f32x4 __attribute__((ext_vector_type(4)));
typedef float f32x2 __attribute__((ext_vector_type(2)));
typedef unsigned u32x4 __attribute__((ext_vector_type(4)));
typedef unsigned long long u64;
#define LAS __attribute__((address_space(3)))
#define GAS __attribute__((address_space(1)))
#define DI __device__ __forceinline__
#define SBAR() __builtin_amdgcn_sched_barrier(0)
#define PIN(x) asm volatile("" : "+v"(x))
#define MFMA(a, b, c) __builtin_amdgcn_mfma_f32_32x32x16_bf16((a), (b), (c), 0, 0, 0)

constexpr int SQ = 128, DM = 512, NH = 32, SK = 2048;
constexpr unsigned PS = 32768u;
constexpr unsigned VOFF = 16384u;
constexpr float QSCALE = 0.08838834764831845f * 1.4426950408889634f;
constexpr float SUM_THR = 65536.0f;
constexpr unsigned WS_EPOCH = 0u, WS_FLAG1 = 4096u, WS_FLAG2 = 65536u, WS_OFRAG = 1u << 20, WS_PART = 16u << 20;
constexpr unsigned SPIN_CAP = 1u << 18;

DI unsigned rfl(unsigned x) { return __builtin_amdgcn_readfirstlane(x); }
DI unsigned cvtpk(float lo, float hi) { f32x2 v = {lo, hi}; bf16x2 b = __builtin_convertvector(v, bf16x2); return __builtin_bit_cast(unsigned, b); }
DI bf16x8 lds_rd128(unsigned addr) { return *(const LAS bf16x8*)(size_t)addr; }
DI f32x4 lds_rdf4(unsigned addr) { return *(const LAS f32x4*)(size_t)addr; }
DI bf16x4 lds_rdtr(unsigned addr) { return __builtin_amdgcn_ds_read_tr16_b64_v4bf16((LAS bf16x4*)(size_t)addr); }
DI bf16x8 cat8(bf16x4 a, bf16x4 b) { return __builtin_shufflevector(a, b, 0, 1, 2, 3, 4, 5, 6, 7); }
DI void lds_wr128(unsigned addr, u32x4 v) { *(LAS u32x4*)(size_t)addr = v; }
DI void lds_wrf4(unsigned addr, f32x4 v) { *(LAS f32x4*)(size_t)addr = v; }
DI float swap32_max(float v) { auto r = __builtin_amdgcn_permlane32_swap(__float_as_uint(v), __float_as_uint(v), false, false); return __builtin_fmaxf(__uint_as_float(r[0]), __uint_as_float(r[1])); }
DI float swap32_sum(float v) { auto r = __builtin_amdgcn_permlane32_swap(__float_as_uint(v), __float_as_uint(v), false, false); return __uint_as_float(r[0]) + __uint_as_float(r[1]); }
DI f32x4 quad(const f32x16& v, int q) { return (f32x4){v[4 * q], v[4 * q + 1], v[4 * q + 2], v[4 * q + 3]}; }
DI void poll_flags(const GAS u64* p, u64 tag, bool skip) {
  for (unsigned spin = 0; spin < SPIN_CAP; ++spin) {
    const u64 v = __hip_atomic_load(p, __ATOMIC_RELAXED, __HIP_MEMORY_SCOPE_AGENT);
    if (__all(skip || v == tag)) break;
    __builtin_amdgcn_s_sleep(2);
  }
  asm volatile("" ::: "memory");
}

#define DMA4(srd, voffA, voffB, soff, m0a, par) \
  asm volatile("s_mov_b32 m0, %0\n\ts_nop 0\n\t" \
               "buffer_load_dwordx4 %2, %4, %5 offen nt lds\n\t" \
               "buffer_load_dwordx4 %3, %4, %5 offen offset:2048 nt lds\n\t" \
               "s_mov_b32 m0, %1\n\ts_nop 0\n\t" \
               "buffer_load_dwordx4 %2, %4, %6 offen nt lds\n\t" \
               "buffer_load_dwordx4 %3, %4, %6 offen offset:2048 nt lds" \
               :: "s"(m0a), "s"((m0a) + 1024u), "v"(voffA), "v"(voffB), "s"(srd), "s"((soff) + 128u * (par)), "s"((soff) + 128u - 128u * (par)) : "memory")
#define DMA2(srd, voffA, voffB, soff, m0a) \
  asm volatile("s_mov_b32 m0, %0\n\ts_nop 0\n\t" \
               "buffer_load_dwordx4 %1, %3, %4 offen nt lds\n\t" \
               "buffer_load_dwordx4 %2, %3, %4 offen offset:2048 nt lds" \
               :: "s"(m0a), "v"(voffA), "v"(voffB), "s"(srd), "s"(soff) : "memory")
#define DMA1(pol, srd, voff, soff, m0a) \
  asm volatile("s_mov_b32 m0, %0\n\ts_nop 0\n\tbuffer_load_dwordx4 %1, %2, %3 offen" pol " lds" :: "s"(m0a), "v"(voff), "s"(srd), "s"(soff) : "memory")

#define BAR_ONLY() asm volatile("s_waitcnt lgkmcnt(0)\n\ts_barrier" ::: "memory")
#define WAIT_BAR(N) asm volatile("s_waitcnt vmcnt(" #N ") lgkmcnt(0)\n\ts_barrier" ::: "memory")

__global__ __launch_bounds__(512, 2) void mha_kernel(const char* __restrict__ x, const char* __restrict__ kc, const char* __restrict__ vc,
                                                     const char* __restrict__ wq, const char* __restrict__ wo, char* __restrict__ out, char* ws) {
  __shared__ __attribute__((aligned(1024))) char lds[163840];
  const unsigned tid = threadIdx.x, lane = tid & 63u, l32 = lane & 31u, hi = lane >> 5;
  const unsigned w = rfl(tid >> 6), wq4 = w & 3u, g = w >> 2;
  const unsigned blk = blockIdx.x, xcd = blk & 7u, j = blk >> 3;
  const unsigned b = 4u * (xcd & 1u) + (j >> 3), hg = xcd >> 1, im = j & 7u, h = 8u * hg + im;
  const unsigned bh = b * NH + h;
  const unsigned L0 = (unsigned)(size_t)lds;

  const __amdgpu_buffer_rsrc_t srdX = __builtin_amdgcn_make_buffer_rsrc((void*)(x + (size_t)b * (SQ * DM * 2)), (short)0, SQ * DM * 2, 0x00020000);
  const __amdgpu_buffer_rsrc_t srdW = __builtin_amdgcn_make_buffer_rsrc((void*)(wq + (size_t)h * 256), (short)0, (int)(DM * 8192u - h * 256u), 0x00020000);
  const __amdgpu_buffer_rsrc_t srdK = __builtin_amdgcn_make_buffer_rsrc((void*)(kc + (size_t)bh * (SK * 256)), (short)0, SK * 256, 0x00020000);
  const __amdgpu_buffer_rsrc_t srdV = __builtin_amdgcn_make_buffer_rsrc((void*)(vc + (size_t)bh * (SK * 256)), (short)0, SK * 256, 0x00020000);

  const unsigned sub = lane >> 5, r8 = (lane >> 2) & 7u, slot = lane & 3u;
  const unsigned vx0 = (16u * w + r8) * 1024u + 64u * sub + 16u * (slot ^ (r8 >> 2));
  const unsigned vx1 = (16u * w + 8u + r8) * 1024u + 64u * sub + 16u * (slot ^ (2u + (r8 >> 2)));
  const unsigned vw = (8u * w + r8) * 8192u + 64u * sub + 16u * slot;
  const unsigned crot = h + 2u * b;
#define XB(c) (L0 + ((c) % 5 == 0 ? 3u * PS : (c) % 5 == 1 ? VOFF : (c) % 5 == 2 ? 4u * PS : (c) % 5 == 3 ? 0u : 2u * PS))
#define WB(c) (L0 + ((c) % 5 == 0 ? 3u * PS + VOFF : (c) % 5 == 1 ? PS + VOFF : (c) % 5 == 2 ? 4u * PS + VOFF : (c) % 5 == 3 ? PS : 2u * PS + VOFF))
#define QL(c, k) do { const unsigned cc_ = ((c) + crot) & 7u; \
    if ((k) == 0) DMA1("", srdX, vx0, 128u * cc_, XB(c) + 2048u * w); \
    else if ((k) == 1) DMA1("", srdX, vx1, 128u * cc_, XB(c) + 2048u * w + 1024u); \
    else if ((k) == 2) DMA1("", srdW, vw, 524288u * cc_, WB(c) + 2048u * w); \
    else DMA1("", srdW, vw, 524288u * cc_ + 128u, WB(c) + 2048u * w + 1024u); } while (0)
#define QLOAD(c) do { QL(c, 0); QL(c, 1); QL(c, 2); QL(c, 3); } while (0)
  QLOAD(0);
  asm volatile("s_barrier" ::: "memory");
  QLOAD(1);
  u64 epoch;
  asm volatile("s_load_dwordx2 %0, %1, 0x0\n\ts_waitcnt lgkmcnt(0)" : "=s"(epoch) : "s"(ws + WS_EPOCH + 256u * b) : "memory");
  const u64 tag = epoch * 0x9E3779B97F4A7C15ull + 0xD1B54A32D192ED03ull;
  const unsigned vkA = r8 * 256u + 64u * sub + 16u * (slot ^ (r8 >> 2));
  const unsigned vkB = r8 * 256u + 64u * sub + 16u * (slot ^ (2u + (r8 >> 2)));
  const unsigned vvA = r8 * 256u + 64u * sub + 16u * slot;
  const unsigned wrow = wq4 * 4096u;
#define DA() DMA4(srdK, vkA, vkB, (g ^ 1u) * 16384u + wrow, L0 + (g ^ 1u) * PS + wrow, g ^ 1u)
#define DB(hf) do { if (g) DMA2(srdK, vkA, vkB, 2u * 16384u + wrow + 128u * (hf), L0 + 2u * PS + wrow + 1024u * (hf)); else DMA2(srdV, vvA, vvA, wrow + 128u * (hf), L0 + 2u * PS + VOFF + wrow + 1024u * (hf)); } while (0)
  const __amdgpu_buffer_rsrc_t srdO = __builtin_amdgcn_make_buffer_rsrc((void*)(wo + 128u * im), (short)0, (int)(4096u * 1024u - 128u * im), 0x00020000);
  const unsigned vwo = r8 * 1024u + 64u * sub + 16u * slot;
#define WDMA4(n, ldsa) do { const unsigned so_ = ((8u * hg + ((im + (n)) & 7u)) * 128u + 32u * wq4) * 1024u; \
    DMA1("", srdO, vwo, so_, (ldsa) + wrow); DMA1("", srdO, vwo, so_ + 8192u, (ldsa) + wrow + 1024u); \
    DMA1("", srdO, vwo, so_ + 16384u, (ldsa) + wrow + 2048u); DMA1("", srdO, vwo, so_ + 24576u, (ldsa) + wrow + 3072u); } while (0)

  bf16x8 qf[8];
  {
    f32x16 qa0 = {}, qa1 = {};
    const unsigned xrd0 = 4096u * wq4 + 1024u * (l32 >> 3) + 64u * (l32 & 7u) + 16u * ((0u + hi) ^ ((l32 >> 2) & 3u));
    const unsigned xrd1 = 4096u * wq4 + 1024u * (l32 >> 3) + 64u * (l32 & 7u) + 16u * ((2u + hi) ^ ((l32 >> 2) & 3u));
    const unsigned p = lane & 3u, psw = ((p & 1u) << 1) | (p >> 1);
    const unsigned wrd = 2048u * hi + 64u * ((lane >> 2) & 3u) + 32u * ((lane >> 4) & 1u) + 8u * psw + 1024u * g;
    bf16x8 fx[2][4], fa[2][4], fb[2][4];
#define QREAD1(c, s) do { \
      fx[(c) & 1][s] = lds_rd128(XB(c) + ((s & 1) ? xrd1 : xrd0) + 512u * (s >> 1)); \
      fa[(c) & 1][s] = cat8(lds_rdtr(WB(c) + wrd + 4096u * s), lds_rdtr(WB(c) + wrd + 4096u * s + 256u)); \
      fb[(c) & 1][s] = cat8(lds_rdtr(WB(c) + wrd + 4096u * s + 512u), lds_rdtr(WB(c) + wrd + 4096u * s + 768u)); SBAR(); } while (0)
#define QMM(c, s) do { qa0 = MFMA(fa[(c) & 1][s], fx[(c) & 1][s], qa0); qa1 = MFMA(fb[(c) & 1][s], fx[(c) & 1][s], qa1); SBAR(); } while (0)
#define QSUB(c, s, LD) do { QMM((c) - 1, s); if (LD) { QL((c) + 3, s); SBAR(); } QREAD1(c, s); } while (0)
#define QSTEP(c, N, LD) do { WAIT_BAR(N); QSUB(c, 0, LD); QSUB(c, 1, LD); QSUB(c, 2, LD); QSUB(c, 3, LD); } while (0)
    WAIT_BAR(4); QREAD1(0, 0); QREAD1(0, 1); QREAD1(0, 2); QREAD1(0, 3); QLOAD(2); QLOAD(3);
    QSTEP(1, 8, 1);
    QSTEP(2, 8, 1);
    QSTEP(3, 8, 1);
    QSTEP(4, 8, 1); DA();
    QSTEP(5, 12, 0); DB(0);
    QSTEP(6, 10, 0);
    QSTEP(7, 6, 0); DB(1);
    QMM(7, 0); QMM(7, 1); QMM(7, 2); QMM(7, 3);
    u32x4 mine[4];
#pragma unroll
    for (int sp = 0; sp < 2; ++sp) {
#pragma unroll
      for (int e = 0; e < 4; ++e) {
        mine[sp][e] = cvtpk(qa0[8 * sp + 2 * e] * QSCALE, qa0[8 * sp + 2 * e + 1] * QSCALE);
        mine[2 + sp][e] = cvtpk(qa1[8 * sp + 2 * e] * QSCALE, qa1[8 * sp + 2 * e + 1] * QSCALE);
      }
    }
    const unsigned qx_w = L0 + VOFF + g * PS + 4096u * wq4 + 16u * lane, qx_r = L0 + VOFF + (g ^ 1u) * PS + 4096u * wq4 + 16u * lane;
#pragma unroll
    for (int i = 0; i < 4; ++i) lds_wr128(qx_w + 1024u * i, mine[i]);
    WAIT_BAR(4);
#pragma unroll
    for (int i = 0; i < 4; ++i) {
      qf[i] = __builtin_bit_cast(bf16x8, mine[i]); qf[4 + i] = lds_rd128(qx_r + 1024u * i);
    }
  }
  const unsigned bsw = ((b & 3u) | ((b >> 2) << 4));
  const __amdgpu_buffer_rsrc_t srdF = __builtin_amdgcn_make_buffer_rsrc((void*)(ws + WS_OFRAG + (size_t)(bsw + 4u * hg) * 262144u), (short)0, 262144, 0x00020000);
  const __amdgpu_buffer_rsrc_t srdP = __builtin_amdgcn_make_buffer_rsrc((void*)(ws + WS_PART + (size_t)bsw * 262144u + im * 32768u), (short)0, 4 << 20, 0x00020000);
  const unsigned vl16 = 16u * lane;
  const unsigned sfo = (2u * wq4 + g) * 4096u, spo = hg * 1048576u + sfo;
  GAS u64* flag1 = (GAS u64*)(ws + WS_FLAG1 + (b * 4u + hg) * 1024u);
  GAS u64* flag2 = (GAS u64*)(ws + WS_FLAG2 + (b * 8u + im) * 512u);
  const unsigned krd0 = L0 + 2048u * (l32 >> 3) + 64u * (l32 & 7u) + 16u * ((0u + hi) ^ ((l32 >> 2) & 3u));
  const unsigned krd1 = L0 + 2048u * (l32 >> 3) + 64u * (l32 & 7u) + 16u * ((2u + hi) ^ ((l32 >> 2) & 3u));
  const unsigned trl = 8u * (lane & 3u) + 64u * ((lane >> 2) & 3u) + 32u * ((lane >> 4) & 1u) + 256u * hi;
  const unsigned vrd = L0 + VOFF + trl;
  f32x16 o0 = {}, o1 = {}, o2 = {}, o3 = {}, s0, s1, negm;
  float mref = 0.f, lsum = 0.f;
  float one = 1.0f; PIN(one);
  u32x4 pf[4];
  bf16x8 fr[8];
#define KFR(m, sl) lds_rd128((((m) >> 1) & 1 ? krd1 : krd0) + (sl) + 8192u * ((m) & 1) + 512u * ((m) >> 2))
#define VFR(m, sl) cat8(lds_rdtr(vrd + (sl) + 4096u * ((m) >> 2) + 512u * ((m) & 3)), lds_rdtr(vrd + (sl) + 4096u * ((m) >> 2) + 512u * ((m) & 3) + 2048u))
#define PVBLOCK() do { _Pragma("unroll") for (int m = 0; m < 16; ++m) { \
      if (m + LA < 16) fr[(m + LA) & 7] = VFR(m + LA, sl); \
      const bf16x8 pb = __builtin_bit_cast(bf16x8, pf[m >> 2]); \
      if ((m & 3) == 0) o0 = MFMA(fr[m & 7], pb, o0); \
      else if ((m & 3) == 1) o1 = MFMA(fr[m & 7], pb, o1); \
      else if ((m & 3) == 2) o2 = MFMA(fr[m & 7], pb, o2); \
      else o3 = MFMA(fr[m & 7], pb, o3); \
      SBAR(); } } while (0)
  constexpr int LA = 6;
  unsigned sl = g * PS;
  if (g) {
    if (w == 4u) {
      const char* fp = (lane & 1u) ? (const char*)(flag2 + 16u * hg) : (const char*)(flag1 + 16u * im);
      asm volatile("s_mov_b32 m0, %0\n\ts_nop 0\n\tglobal_load_lds_dword %1, off sc1" :: "s"(L0 + 4u * PS), "v"(fp) : "memory");
    }
    DMA4(srdK, vkA, vkB, 3u * 16384u + wrow, L0 + 3u * PS + wrow, 1u);
    DMA4(srdV, vvA, vvA, 1u * 16384u + wrow, L0 + 3u * PS + VOFF + wrow, 1u);
    BAR_ONLY();
  }
#define QKBLOCK(CINIT) do { \
    _Pragma("unroll") for (int m = 0; m < LA; ++m) fr[m] = KFR(m, sl); \
    SBAR(); \
    _Pragma("unroll") for (int m = 0; m < 16; ++m) { \
      if (m + LA < 16) fr[(m + LA) & 7] = KFR(m + LA, sl); \
      else fr[(m + LA) & 7] = VFR(m + LA - 16, sl); \
      if (m == 0) s0 = MFMA(fr[0], qf[0], CINIT); \
      else if (m == 1) s1 = MFMA(fr[1], qf[0], CINIT); \
      else if (m & 1) s1 = MFMA(fr[m & 7], qf[m >> 1], s1); \
      else s0 = MFMA(fr[m & 7], qf[m >> 1], s0); \
      SBAR(); } } while (0)
#define EXPS() do { float a0 = 0.f, a1 = 0.f; \
      _Pragma("unroll") for (int e = 0; e < 8; ++e) { const float e0 = __builtin_amdgcn_exp2f(s0[2 * e]), e1 = __builtin_amdgcn_exp2f(s0[2 * e + 1]); a0 += e0; a1 = __builtin_fmaf(e1, one, a1); pf[e >> 2][e & 3] = cvtpk(e0, e1); } \
      _Pragma("unroll") for (int e = 0; e < 8; ++e) { const float e0 = __builtin_amdgcn_exp2f(s1[2 * e]), e1 = __builtin_amdgcn_exp2f(s1[2 * e + 1]); a0 += e0; a1 = __builtin_fmaf(e1, one, a1); pf[2 + (e >> 2)][e & 3] = cvtpk(e0, e1); } \
      ts = a0 + a1; } while (0)
#define ROWMAX(mx) do { mx = s0[0]; \
      _Pragma("unroll") for (int e = 1; e < 16; ++e) mx = __builtin_fmaxf(mx, s0[e]); \
      _Pragma("unroll") for (int e = 0; e < 16; ++e) mx = __builtin_fmaxf(mx, s1[e]); \
      mx = swap32_max(mx); } while (0)
#define SHIFT(dl) do { _Pragma("unroll") for (int e = 0; e < 16; e += 2) { \
      s0[e] -= dl; s0[e + 1] = __builtin_fmaf(s0[e + 1], one, -dl); s1[e] -= dl; s1[e + 1] = __builtin_fmaf(s1[e + 1], one, -dl); } } while (0)
  float ts;
  {
    const f32x16 zero = {};
    QKBLOCK(zero);
    __builtin_amdgcn_s_setprio(1);
    if (g) { WAIT_BAR(8); } else { BAR_ONLY(); }
    DMA4(srdK, vkA, vkB, (g + 4u) * 16384u + wrow, L0 + sl + wrow, g);
    ROWMAX(mref);
    SHIFT(mref);
#pragma unroll
    for (int e = 0; e < 16; ++e) negm[e] = -mref;
    PIN(negm);
    EXPS();
    lsum = ts;
    PIN(pf[0]); PIN(pf[1]); PIN(pf[2]); PIN(pf[3]); PIN(lsum);
    SBAR();
    DMA4(srdV, vvA, vvA, (g + 2u) * 16384u + wrow, L0 + sl + VOFF + wrow, g);
    sl ^= 2u * PS;
    WAIT_BAR(8);
  }
  for (unsigned t = 1; t < 16; ++t) {
    const unsigned i = 2u * t + g;
    __builtin_amdgcn_s_setprio(0);
    QKBLOCK(negm);
    PVBLOCK();
    __builtin_amdgcn_s_setprio(1);
    BAR_ONLY();
    const unsigned pn = i + 4u;
    if (pn <= 31u) DMA4(srdK, vkA, vkB, pn * 16384u + wrow, L0 + sl + wrow, g);
    else if (pn == 32u) DMA4(srdV, vvA, vvA, 30u * 16384u + wrow, L0 + sl + VOFF + wrow, 0u);
#define ISSUE2() do { if (pn <= 31u) DMA4(srdV, vvA, vvA, (pn - 2u) * 16384u + wrow, L0 + sl + VOFF + wrow, g); \
    else if (pn == 32u) DMA4(srdV, vvA, vvA, 31u * 16384u + wrow, L0 + sl + wrow, 1u);     \
    else if (pn == 33u) { WDMA4(0u, L0 + PS); WDMA4(1u, L0 + PS + VOFF); } \
    else if (pn == 34u) { WDMA4(2u, L0 + 4u * PS); WDMA4(3u, L0 + 4u * PS + VOFF); } } while (0)
    EXPS();
    if (__builtin_expect(__any(!(ts <= SUM_THR)), 0)) {
      float mx; ROWMAX(mx);
      const float dl = __builtin_fmaxf(mx, 0.f);
      const float f = __builtin_amdgcn_exp2f(-dl);
      mref += dl; lsum *= f;
      SHIFT(dl);
#pragma unroll
      for (int e = 0; e < 16; ++e) { o0[e] *= f; o1[e] *= f; o2[e] *= f; o3[e] *= f; negm[e] = -mref; }
      PIN(negm);
      EXPS();
    }
    lsum += ts;
    PIN(pf[0]); PIN(pf[1]); PIN(pf[2]); PIN(pf[3]); PIN(lsum);
    SBAR();
    ISSUE2();
    sl ^= 2u * PS;
    WAIT_BAR(8);
  }
  __builtin_amdgcn_s_setprio(0);
  u32x4 wreg[8];
  u32x4 own[4], b0[4], b1[4], b2[4];
  const unsigned xr = (2u + g) * PS + 8192u * wq4;
  {
    const unsigned xw = L0 + (3u - g) * PS + 8192u * wq4 + vl16;
    unsigned vf = vrd - g * VOFF; PIN(vf);
#define VFL(m) cat8(lds_rdtr(vf + 4096u * (((m) >> 1) & 3) + 512u * (((m) & 1) + ((m) < 8 ? 2 : 0))), lds_rdtr(vf + 4096u * (((m) >> 1) & 3) + 512u * (((m) & 1) + ((m) < 8 ? 2 : 0)) + 2048u))
#pragma unroll
    for (int m = 0; m < LA; ++m) fr[m] = VFL(m);
    SBAR();
    const unsigned swo = ((8u * hg + ((im + 4u + (w >> 1)) & 7u)) * 128u + 64u * (w & 1u)) * 1024u;
#pragma unroll
    for (int m = 0; m < 16; ++m) {
      if (m + LA < 16) fr[(m + LA) & 7] = VFL(m + LA);
      if (m < 8) wreg[m] = __builtin_amdgcn_raw_buffer_load_b128(srdO, vwo, swo + 8192u * m, 0);
      const bf16x8 pb = __builtin_bit_cast(bf16x8, pf[(m >> 1) & 3]);
      if (m < 8) { if (m & 1) o3 = MFMA(fr[m & 7], pb, o3); else o2 = MFMA(fr[m & 7], pb, o2); }
      else       { if (m & 1) o1 = MFMA(fr[m & 7], pb, o1); else o0 = MFMA(fr[m & 7], pb, o0); }
      if (m >= 10 && m < 14) { lds_wrf4(xw + 1024u * (m - 10), quad(o2, m - 10)); lds_wrf4(xw + 4096u + 1024u * (m - 10), quad(o3, m - 10)); }
      SBAR();
      if (m == 9 && !g) { BAR_ONLY(); }
    }
    lsum = swap32_sum(lsum);
    *(LAS f32x2*)(size_t)(L0 + VOFF + 512u * w + 8u * lane) = (f32x2){mref, lsum};
    if (g) { WAIT_BAR(8); } else { BAR_ONLY(); }
    f32x4 oth[8];
#pragma unroll
    for (int q = 0; q < 8; ++q) oth[q] = lds_rdf4(L0 + xr + vl16 + 1024u * q);
    const f32x2 st = *(const LAS f32x2*)(size_t)(L0 + VOFF + 512u * (w ^ 4u) + 8u * lane);
    const float mm = __builtin_fmaxf(mref, st[0]);
    const float fa = __builtin_amdgcn_exp2f(mref - mm), fb = __builtin_amdgcn_exp2f(st[0] - mm);
    const float inv = 1.0f / (fa * lsum + fb * st[1]);
    const float ca = fa * inv, cb = fb * inv;
#pragma unroll
    for (int d = 0; d < 2; ++d) {
#pragma unroll
      for (int sp = 0; sp < 2; ++sp) {
        const f32x4 u0 = oth[4 * d + 2 * sp], u1 = oth[4 * d + 2 * sp + 1];
        const f32x16& mo = d ? o1 : o0;
        u32x4 r;
        r[0] = cvtpk(mo[8 * sp + 0] * ca + u0[0] * cb, mo[8 * sp + 1] * ca + u0[1] * cb);
        r[1] = cvtpk(mo[8 * sp + 2] * ca + u0[2] * cb, mo[8 * sp + 3] * ca + u0[3] * cb);
        r[2] = cvtpk(mo[8 * sp + 4] * ca + u1[0] * cb, mo[8 * sp + 5] * ca + u1[1] * cb);
        r[3] = cvtpk(mo[8 * sp + 6] * ca + u1[2] * cb, mo[8 * sp + 7] * ca + u1[3] * cb);
        own[2 * d + sp] = r;
        __builtin_amdgcn_raw_buffer_store_b128(r, srdF, vl16 + 1024u * (2 * d + sp), im * 32768u + sfo, 16);
      }
    }
  }
  SBAR();
  f32x16 c0 = {}, c1 = {};
  const unsigned ard0 = L0 + trl + 8192u * g + 512u * g, ard1 = L0 + trl + 8192u * g + 512u * (g ^ 1u);
  bf16x8 aa[8], ab[8];
  unsigned rdy = 0u;
  const GAS u64* hpoll = flag1 + 16u * im + ((im + lane) & 7u);
#define UB(n) ((n) < 2 ? PS + (n) * VOFF : (n) < 4 ? 4u * PS + ((n) - 2u) * VOFF : (n) * VOFF)
#define ARD1(A, u0_, u1_, ks) do { A[2 * (ks)] = cat8(lds_rdtr(u0_ + 2048u * (ks)), lds_rdtr(u0_ + 2048u * (ks) + 1024u)); \
      A[2 * (ks) + 1] = cat8(lds_rdtr(u1_ + 2048u * (ks)), lds_rdtr(u1_ + 2048u * (ks) + 1024u)); } while (0)
#define OLOAD(B, n) do { const unsigned so_ = ((im + (n)) & 7u) * 32768u + sfo; \
    _Pragma("unroll") for (int k_ = 0; k_ < 4; ++k_) B[k_] = __builtin_amdgcn_raw_buffer_load_b128(srdF, vl16 + 1024u * k_, so_, 16); SBAR(); } while (0)
#define PROC(A, B, AN, nn) do { unsigned u0_ = ard0 + UB(nn), u1_ = ard1 + UB(nn); PIN(u0_); PIN(u1_); \
    _Pragma("unroll") for (int ks = 0; ks < 4; ++ks) { \
      c0 = MFMA(A[2 * ks], __builtin_bit_cast(bf16x8, B[ks]), c0); c1 = MFMA(A[2 * ks + 1], __builtin_bit_cast(bf16x8, B[ks]), c1); SBAR(); \
      ARD1(AN, u0_, u1_, ks); SBAR(); } } while (0)
#define PROCL(A, B) do { _Pragma("unroll") for (int ks = 0; ks < 4; ++ks) { \
      c0 = MFMA(A[2 * ks], __builtin_bit_cast(bf16x8, B[ks]), c0); c1 = MFMA(A[2 * ks + 1], __builtin_bit_cast(bf16x8, B[ks]), c1); } SBAR(); } while (0)
#define NEED(n) do { unsigned spin_ = 0; \
    while (!((rdy >> (n)) & 1u) && ++spin_ < SPIN_CAP) { \
      rdy = (unsigned)__ballot(__hip_atomic_load(hpoll, __ATOMIC_RELAXED, __HIP_MEMORY_SCOPE_AGENT) == tag); \
      if ((rdy >> (n)) & 1u) break; __builtin_amdgcn_s_sleep(2); } \
    asm volatile("" ::: "memory"); } while (0)
  { unsigned u0_ = ard0 + UB(0), u1_ = ard1 + UB(0); PIN(u0_); PIN(u1_); ARD1(aa, u0_, u1_, 0); ARD1(aa, u0_, u1_, 1); ARD1(aa, u0_, u1_, 2); ARD1(aa, u0_, u1_, 3); SBAR(); }
  PROC(aa, own, ab, 1);
#pragma unroll
  for (int q = 0; q < 8; ++q) lds_wr128(L0 + xr + vl16 + 1024u * q, wreg[q]);
  WAIT_BAR(0);
  if (tid < 8u) __hip_atomic_store(flag1 + 16u * tid + im, tag, __ATOMIC_RELAXED, __HIP_MEMORY_SCOPE_AGENT);
  NEED(1); OLOAD(b0, 1);
  NEED(2); OLOAD(b1, 2);
  NEED(3); OLOAD(b2, 3);
  PROC(ab, b0, aa, 2);
  NEED(4); OLOAD(b0, 4);
  PROC(aa, b1, ab, 3);
  NEED(5); OLOAD(b1, 5);
  PROC(ab, b2, aa, 4);
  NEED(6); OLOAD(b2, 6);
  PROC(aa, b0, ab, 5);
  NEED(7); OLOAD(b0, 7);
  PROC(ab, b1, aa, 6);
  PROC(aa, b2, ab, 7);
  PROCL(ab, b0);
  {
    const unsigned ra = L0 + g * VOFF + 4096u * wq4 + vl16, rb = L0 + (g ^ 1u) * VOFF + 4096u * wq4 + vl16;
#pragma unroll
    for (int q = 0; q < 4; ++q) lds_wrf4(ra + 1024u * q, quad(c1, q));
    BAR_ONLY();
    f32x4 kp[4];
#pragma unroll
    for (int q = 0; q < 4; ++q) kp[q] = quad(c0, q) + lds_rdf4(rb + 1024u * q);
    if (wq4 != hg) {
#pragma unroll
      for (int q = 0; q < 4; ++q) __builtin_amdgcn_raw_buffer_store_b128(__builtin_bit_cast(u32x4, kp[q]), srdP, vl16 + 1024u * q, spo, 16);
    }
    WAIT_BAR(0);
    if (w == ((hg + 1u) & 3u) && lane < 4u) __hip_atomic_store(flag2 + 16u * lane + hg, tag, __ATOMIC_RELAXED, __HIP_MEMORY_SCOPE_AGENT);
    if (wq4 == hg) {
      poll_flags(flag2 + 16u * hg + (lane & 3u), tag, (lane & 3u) == hg);
      u32x4 p0[4], p1[4], p2[4];
#pragma unroll
      for (int q = 0; q < 4; ++q) {
        p0[q] = __builtin_amdgcn_raw_buffer_load_b128(srdP, vl16 + 1024u * q, sfo + ((hg + 1u) & 3u) * 1048576u, 16);
        p1[q] = __builtin_amdgcn_raw_buffer_load_b128(srdP, vl16 + 1024u * q, sfo + ((hg + 2u) & 3u) * 1048576u, 16);
        p2[q] = __builtin_amdgcn_raw_buffer_load_b128(srdP, vl16 + 1024u * q, sfo + ((hg + 3u) & 3u) * 1048576u, 16);
      }
      if (h == 0u && tid == 0u)
        __hip_atomic_store((GAS u64*)(ws + WS_EPOCH + 256u * b), epoch + 1u, __ATOMIC_RELAXED, __HIP_MEMORY_SCOPE_AGENT);
      unsigned pk[8];
#pragma unroll
      for (int q = 0; q < 4; ++q) {
        const f32x4 a = kp[q] + __builtin_bit_cast(f32x4, p0[q]) + __builtin_bit_cast(f32x4, p1[q]) + __builtin_bit_cast(f32x4, p2[q]);
        pk[2 * q] = cvtpk(a[0], a[1]); pk[2 * q + 1] = cvtpk(a[2], a[3]);
      }
      char* dst = out + ((size_t)(b * SQ + 32u * hg + l32) * DM + 64u * im + 32u * g) * 2u + 16u * hi;
#pragma unroll
      for (int q = 0; q < 4; q += 2) {
        auto r0 = __builtin_amdgcn_permlane32_swap(pk[2 * q], pk[2 * q + 2], false, false);
        auto r1 = __builtin_amdgcn_permlane32_swap(pk[2 * q + 1], pk[2 * q + 3], false, false);
        *(u32x4*)(dst + 16 * q) = (u32x4){r0[0], r1[0], r0[1], r1[1]};
      }
    }
  }
}

extern "C" void kernel_launch(void* const* d_in, const int* in_sizes, int n_in, void* d_out, int out_size, void* d_ws, size_t ws_size, hipStream_t stream) {
  mha_kernel<<<256, 512, 0, stream>>>((const char*)d_in[0], (const char*)d_in[1], (const char*)d_in[2], (const char*)d_in[3], (const char*)d_in[4], (char*)d_out, (char*)d_ws);
}
```

```cpp
#include <hip/hip_runtime.h>
#include <hip/hip_bf16.h>

typedef __bf16 bf16x8 __attribute__((ext_vector_type(8)));
typedef __bf16 bf16x4 __attribute__((ext_vector_type(4)));
typedef __bf16 bf16x2 __attribute__((ext_vector_type(2)));
typedef float f32x16 __attribute__((ext_vector_type(16)));
typedef float f32x4 __attribute__((ext_vector_type(4)));
typedef float f32x2 __attribute__((ext_vector_type(2)));
typedef unsigned u32x4 __attribute__((ext_vector_type(4)));
typedef unsigned long long u64;
#define LAS __attribute__((address_space(3)))
#define GAS __attribute__((address_space(1)))
#define DI __device__ __forceinline__
#define SBAR() __builtin_amdgcn_sched_barrier(0)
#define PIN(x) asm volatile("" : "+v"(x))
#define MFMA(a, b, c) __builtin_amdgcn_mfma_f32_32x32x16_bf16((a), (b), (c), 0, 0, 0)
#define MF16(a, b, c) __builtin_amdgcn_mfma_f32_16x16x32_bf16((a), (b), (c), 0, 0, 0)
#define BF8(x) __builtin_bit_cast(bf16x8, (x))

#define PRIO_M 0
#define PRIO_S 1
#define PRETOUCH 1
#define EXP_OOB 0u
#define SPLIT_ISSUE 1
#define WO_AUX 0
#define XW_POL ""
#define WO_POL ""
#define WS_BASE 0u
constexpr int NB = 8, SQ = 128, DM = 512, NH = 32, SK = 2048, DH = 128;
constexpr unsigned PS = 32768u;
constexpr unsigned VOFF = 16384u;
constexpr float QSCALE = 0.08838834764831845f * 1.4426950408889634f;
constexpr float SUM_THR = 65536.0f;
constexpr unsigned WS_EPOCH = 0u, WS_FLAG1 = 4096u, WS_FLAG2 = 65536u, WS_OFRAG = 1u << 20, WS_PART = 16u << 20;
constexpr unsigned SPIN_CAP = 1u << 18;

DI unsigned rfl(unsigned x) { return __builtin_amdgcn_readfirstlane(x); }
DI unsigned cvtpk(float lo, float hi) { f32x2 v = {lo, hi}; bf16x2 b = __builtin_convertvector(v, bf16x2); return __builtin_bit_cast(unsigned, b); }
DI bf16x8 lds_rd128(unsigned addr) { return *(const LAS bf16x8*)(size_t)addr; }
DI f32x4 lds_rdf4(unsigned addr) { return *(const LAS f32x4*)(size_t)addr; }
DI bf16x4 lds_rdtr(unsigned addr) { return __builtin_amdgcn_ds_read_tr16_b64_v4bf16((LAS bf16x4*)(size_t)addr); }
DI bf16x8 cat8(bf16x4 a, bf16x4 b) { return __builtin_shufflevector(a, b, 0, 1, 2, 3, 4, 5, 6, 7); }
DI void lds_wr128(unsigned addr, u32x4 v) { *(LAS u32x4*)(size_t)addr = v; }
DI void lds_wrf4(unsigned addr, f32x4 v) { *(LAS f32x4*)(size_t)addr = v; }
DI float grp_max(float v) {
  auto r = __builtin_amdgcn_permlane32_swap(__float_as_uint(v), __float_as_uint(v), false, false); v = __builtin_fmaxf(__uint_as_float(r[0]), __uint_as_float(r[1]));
  auto t = __builtin_amdgcn_permlane16_swap(__float_as_uint(v), __float_as_uint(v), false, false); return __builtin_fmaxf(__uint_as_float(t[0]), __uint_as_float(t[1])); }
DI float grp_sum(float v) {
  auto r = __builtin_amdgcn_permlane32_swap(__float_as_uint(v), __float_as_uint(v), false, false); v = __uint_as_float(r[0]) + __uint_as_float(r[1]);
  auto t = __builtin_amdgcn_permlane16_swap(__float_as_uint(v), __float_as_uint(v), false, false); return __uint_as_float(t[0]) + __uint_as_float(t[1]); }
DI f32x4 quad(const f32x16& v, int q) { return (f32x4){v[4 * q], v[4 * q + 1], v[4 * q + 2], v[4 * q + 3]}; }
DI void poll_flags(const GAS u64* p, u64 tag, bool skip) {
  for (unsigned spin = 0; spin < SPIN_CAP; ++spin) {
    const u64 v = __hip_atomic_load(p, __ATOMIC_RELAXED, __HIP_MEMORY_SCOPE_AGENT);
    if (__all(skip || v == tag)) break;
    __builtin_amdgcn_s_sleep(2);
  }
  asm volatile("" ::: "memory");
}

#define DMA4(srd, voffA, voffB, soff, m0a, par) \
  asm volatile("s_mov_b32 m0, %0\n\ts_nop 0\n\t" \
               "buffer_load_dwordx4 %2, %4, %5 offen nt lds\n\t" \
               "buffer_load_dwordx4 %3, %4, %5 offen offset:2048 nt lds\n\t" \
               "s_mov_b32 m0, %1\n\ts_nop 0\n\t" \
               "buffer_load_dwordx4 %2, %4, %6 offen nt lds\n\t" \
               "buffer_load_dwordx4 %3, %4, %6 offen offset:2048 nt lds" \
               :: "s"(m0a), "s"((m0a) + 1024u), "v"(voffA), "v"(voffB), "s"(srd), "s"((soff) + 128u * (par)), "s"((soff) + 128u - 128u * (par)) : "memory")
#define DMA2(srd, voffA, voffB, soff, m0a) \
  asm volatile("s_mov_b32 m0, %0\n\ts_nop 0\n\t" \
               "buffer_load_dwordx4 %1, %3, %4 offen nt lds\n\t" \
               "buffer_load_dwordx4 %2, %3, %4 offen offset:2048 nt lds" \
               :: "s"(m0a), "v"(voffA), "v"(voffB), "s"(srd), "s"(soff) : "memory")
#define DMA1(pol, srd, voff, soff, m0a) \
  asm volatile("s_mov_b32 m0, %0\n\ts_nop 0\n\tbuffer_load_dwordx4 %1, %2, %3 offen" pol " lds" :: "s"(m0a), "v"(voff), "s"(srd), "s"(soff) : "memory")

#define STAMP(i)
#define STAMPC(i)
#define BAR_ONLY() asm volatile("s_waitcnt lgkmcnt(0)\n\ts_barrier" ::: "memory")
#define WAIT_BAR(N) asm volatile("s_waitcnt vmcnt(" #N ") lgkmcnt(0)\n\ts_barrier" ::: "memory")

__global__ __launch_bounds__(512, 2) void mha_kernel(const char* __restrict__ x, const char* __restrict__ kc, const char* __restrict__ vc,
                                                     const char* __restrict__ wq, const char* __restrict__ wo, char* __restrict__ out, char* ws) {
  __shared__ __attribute__((aligned(1024))) char lds[163840];
  const unsigned tid = threadIdx.x, lane = tid & 63u, l32 = lane & 31u, hi = lane >> 5;
  const unsigned w = rfl(tid >> 6), wq4 = w & 3u, g = w >> 2;
  const unsigned blk = blockIdx.x, xcd = blk & 7u, j = blk >> 3;
  const unsigned b = 4u * (xcd & 1u) + (j >> 3), hg = xcd >> 1, im = j & 7u, h = 8u * hg + im;
  const unsigned bh = b * NH + h;
  const unsigned L0 = (unsigned)(size_t)lds;
  STAMP(0); STAMPC(19);

  const __amdgpu_buffer_rsrc_t srdX = __builtin_amdgcn_make_buffer_rsrc((void*)(x + (size_t)b * (SQ * DM * 2)), (short)0, SQ * DM * 2, 0x00020000);
  const __amdgpu_buffer_rsrc_t srdW = __builtin_amdgcn_make_buffer_rsrc((void*)(wq + (size_t)h * 256), (short)0, (int)(DM * 8192u - h * 256u), 0x00020000);
  const __amdgpu_buffer_rsrc_t srdK = __builtin_amdgcn_make_buffer_rsrc((void*)(kc + (size_t)bh * (SK * 256)), (short)0, SK * 256, 0x00020000);
  const __amdgpu_buffer_rsrc_t srdV = __builtin_amdgcn_make_buffer_rsrc((void*)(vc + (size_t)bh * (SK * 256)), (short)0, SK * 256, 0x00020000);

  const unsigned sub = lane >> 5, r8 = (lane >> 2) & 7u, slot = lane & 3u;
  const unsigned vx0 = (16u * w + r8) * 1024u + 64u * sub + 16u * (slot ^ (r8 >> 2));
  const unsigned vx1 = (16u * w + 8u + r8) * 1024u + 64u * sub + 16u * (slot ^ (2u + (r8 >> 2)));
  const unsigned vw = (8u * w + r8) * 8192u + 64u * sub + 16u * slot;
  const unsigned crot = h + 2u * b;
#define XB(c) (L0 + ((c) % 5 == 0 ? 3u * PS : (c) % 5 == 1 ? VOFF : (c) % 5 == 2 ? 4u * PS : (c) % 5 == 3 ? 0u : 2u * PS))
#define WB(c) (L0 + ((c) % 5 == 0 ? 3u * PS + VOFF : (c) % 5 == 1 ? PS + VOFF : (c) % 5 == 2 ? 4u * PS + VOFF : (c) % 5 == 3 ? PS : 2u * PS + VOFF))
#define QL(c, k) do { const unsigned cc_ = ((c) + crot) & 7u; \
    if ((k) == 0) DMA1(XW_POL, srdX, vx0, 128u * cc_, XB(c) + 2048u * w); \
    else if ((k) == 1) DMA1(XW_POL, srdX, vx1, 128u * cc_, XB(c) + 2048u * w + 1024u); \
    else if ((k) == 2) DMA1(XW_POL, srdW, vw, 524288u * cc_, WB(c) + 2048u * w); \
    else DMA1(XW_POL, srdW, vw, 524288u * cc_ + 128u, WB(c) + 2048u * w + 1024u); } while (0)
#define QLOAD(c) do { QL(c, 0); QL(c, 1); QL(c, 2); QL(c, 3); } while (0)
  QLOAD(0);
  asm volatile("s_barrier" ::: "memory");
  QLOAD(1);
  u64 epoch;
  asm volatile("s_load_dwordx2 %0, %1, 0x0\n\ts_waitcnt lgkmcnt(0)" : "=s"(epoch) : "s"(ws + WS_EPOCH + 256u * b) : "memory");
  const u64 tag = epoch * 0x9E3779B97F4A7C15ull + 0xD1B54A32D192ED03ull;
  const unsigned vkA = r8 * 256u + 64u * sub + 16u * (slot ^ (2u * (r8 >> 2)));
  const unsigned vkB = r8 * 256u + 64u * sub + 16u * (slot ^ (3u ^ (2u * (r8 >> 2))));
  const unsigned vvA = vkA;
  const unsigned wrow = wq4 * 4096u;
#define DA() DMA4(srdK, vkA, vkB, (g ^ 1u) * 16384u + wrow, L0 + (g ^ 1u) * PS + wrow, 0u)
#define DB(hf) do { if (g) DMA2(srdK, vkA, vkB, 2u * 16384u + wrow + 128u * (hf), L0 + 2u * PS + wrow + 1024u * (hf)); else DMA2(srdV, vvA, vvA, wrow + 128u * (hf), L0 + 2u * PS + VOFF + wrow + 1024u * (hf)); } while (0)
  const __amdgpu_buffer_rsrc_t srdO = __builtin_amdgcn_make_buffer_rsrc((void*)(wo + 128u * im), (short)0, (int)(4096u * 1024u - 128u * im), 0x00020000);
  const unsigned vwo = r8 * 1024u + 64u * sub + 16u * (slot ^ (2u * (r8 >> 2)));
#define WDMA4(n, ldsa) do { const unsigned so_ = ((8u * hg + ((im + (n)) & 7u)) * 128u + 32u * wq4) * 1024u; \
    DMA1(WO_POL, srdO, vwo, so_, (ldsa) + wrow); DMA1(WO_POL, srdO, vwo, so_ + 8192u, (ldsa) + wrow + 1024u); \
    DMA1(WO_POL, srdO, vwo, so_ + 16384u, (ldsa) + wrow + 2048u); DMA1(WO_POL, srdO, vwo, so_ + 24576u, (ldsa) + wrow + 3072u); } while (0)

  STAMP(1);
  bf16x8 qf[2][4];
  const unsigned l16 = lane & 15u, g4 = lane >> 4;
  {
    f32x16 qa0 = {}, qa1 = {};
    const unsigned xrd0 = 4096u * wq4 + 1024u * (l32 >> 3) + 64u * (l32 & 7u) + 16u * ((0u + hi) ^ ((l32 >> 2) & 3u));
    const unsigned xrd1 = 4096u * wq4 + 1024u * (l32 >> 3) + 64u * (l32 & 7u) + 16u * ((2u + hi) ^ ((l32 >> 2) & 3u));
    const unsigned p = lane & 3u, psw = ((p & 1u) << 1) | (p >> 1);
    const unsigned wrd = 2048u * hi + 64u * ((lane >> 2) & 3u) + 32u * ((lane >> 4) & 1u) + 8u * psw + 1024u * g;
    bf16x8 fx[2][4], fa[2][4], fb[2][4];
#define QREAD1(c, s) do { \
      fx[(c) & 1][s] = lds_rd128(XB(c) + ((s & 1) ? xrd1 : xrd0) + 512u * (s >> 1)); \
      fa[(c) & 1][s] = cat8(lds_rdtr(WB(c) + wrd + 4096u * s), lds_rdtr(WB(c) + wrd + 4096u * s + 256u)); \
      fb[(c) & 1][s] = cat8(lds_rdtr(WB(c) + wrd + 4096u * s + 512u), lds_rdtr(WB(c) + wrd + 4096u * s + 768u)); SBAR(); } while (0)
#define QMM(c, s) do { qa0 = MFMA(fa[(c) & 1][s], fx[(c) & 1][s], qa0); qa1 = MFMA(fb[(c) & 1][s], fx[(c) & 1][s], qa1); SBAR(); } while (0)
#define QSUB(c, s, LD) do { QMM((c) - 1, s); if (LD) { QL((c) + 3, s); SBAR(); } QREAD1(c, s); } while (0)
#define QSTEP(c, N, LD) do { WAIT_BAR(N); QSUB(c, 0, LD); QSUB(c, 1, LD); QSUB(c, 2, LD); QSUB(c, 3, LD); } while (0)
    WAIT_BAR(4); QREAD1(0, 0); QREAD1(0, 1); QREAD1(0, 2); QREAD1(0, 3); QLOAD(2); QLOAD(3); STAMP(2);
    QSTEP(1, 8, 1);
    QSTEP(2, 8, 1);
    QSTEP(3, 8, 1); STAMP(3);
    QSTEP(4, 8, 1); DA();
    QSTEP(5, 12, 0); DB(0);
    QSTEP(6, 10, 0);
    QSTEP(7, 6, 0); DB(1);
    QMM(7, 0); QMM(7, 1); QMM(7, 2); QMM(7, 3); STAMP(4);
    u32x4 mine[4];
#pragma unroll
    for (int sp = 0; sp < 2; ++sp) {
#pragma unroll
      for (int e = 0; e < 4; ++e) {
        mine[sp][e] = cvtpk(qa0[8 * sp + 2 * e] * QSCALE, qa0[8 * sp + 2 * e + 1] * QSCALE);
        mine[2 + sp][e] = cvtpk(qa1[8 * sp + 2 * e] * QSCALE, qa1[8 * sp + 2 * e + 1] * QSCALE);
      }
    }
    const unsigned qx = L0 + VOFF + (wq4 >> 1) * PS + (wq4 & 1u) * 8192u;
#pragma unroll
    for (int i = 0; i < 4; ++i) lds_wr128(qx + 4096u * g + 1024u * i + 16u * lane, mine[i]);
    WAIT_BAR(4);
#pragma unroll
    for (int ds = 0; ds < 4; ++ds) { qf[0][ds] = lds_rd128(qx + 512u * g4 + 16u * l16 + 2048u * ds); qf[1][ds] = lds_rd128(qx + 512u * g4 + 16u * l16 + 2048u * ds + 256u); }
  }
  STAMP(5); STAMPC(20);
  const unsigned bsw = ((b & 3u) | ((b >> 2) << 4));
  const __amdgpu_buffer_rsrc_t srdF = __builtin_amdgcn_make_buffer_rsrc((void*)(ws + WS_OFRAG + (size_t)(bsw + 4u * hg) * 262144u), (short)0, 262144, 0x00020000);
  const __amdgpu_buffer_rsrc_t srdP = __builtin_amdgcn_make_buffer_rsrc((void*)(ws + WS_PART + (size_t)bsw * 262144u + im * 32768u), (short)0, 4 << 20, 0x00020000);
  const unsigned vl16 = 16u * lane;
  const unsigned sfo = (2u * wq4 + g) * 4096u, spo = hg * 1048576u + sfo;
  GAS u64* flag1 = (GAS u64*)(ws + WS_FLAG1 + (b * 4u + hg) * 1024u);
  GAS u64* flag2 = (GAS u64*)(ws + WS_FLAG2 + (b * 8u + im) * 512u);
  const unsigned q2 = l16 >> 2, sw = ((q2 << 1) ^ (3u * (q2 >> 1))) & 3u;
  const unsigned krd = L0 + 2048u * (l16 >> 3) + 64u * (l16 & 7u) + 16u * (g4 ^ sw);
  const unsigned trl = 256u * (g4 & 1u) + 64u * q2 + 32u * (g4 & 1u) + 8u * (lane & 3u);
  const unsigned vrdE = L0 + VOFF + 2048u * (g4 >> 1) + trl, vrdO = vrdE ^ 32u;
  f32x4 oa[8][2] = {}, sa[4][2], negm[2];
  float mref0 = 0.f, mref1 = 0.f, lsum0 = 0.f, lsum1 = 0.f;
  float one = 1.0f; PIN(one);
  u32x4 pf[2][2];
  bf16x8 fr[8];
#define KFR(m, sl) lds_rd128(krd + (sl) + 4096u * ((m) & 3) + 512u * ((m) >> 2))
#define VFX(vE, vO, db, ks) cat8(lds_rdtr((((db) & 1) ? vO : vE) + 8192u * (ks) + 512u * ((db) >> 1)), lds_rdtr((((db) & 1) ? vO : vE) + 8192u * (ks) + 512u * ((db) >> 1) + 4096u))
#define VFR(m, sl) VFX(vrdE + (sl), vrdO + (sl), (m) & 7, (m) >> 3)
#define PVBLOCK() do { _Pragma("unroll") for (int m = 0; m < 16; ++m) { \
      if (m + LA < 16) fr[(m + LA) & 7] = VFR(m + LA, sl); \
      oa[m & 7][0] = MF16(fr[m & 7], BF8(pf[0][m >> 3]), oa[m & 7][0]); oa[m & 7][1] = MF16(fr[m & 7], BF8(pf[1][m >> 3]), oa[m & 7][1]); \
      SBAR(); } } while (0)
  constexpr int LA = 6;
  unsigned sl = g * PS;
  if (g) {
    if (w == 4u) {
      const char* fp = (lane & 1u) ? (const char*)(flag2 + 16u * hg) : (const char*)(flag1 + 16u * im);
      asm volatile("s_mov_b32 m0, %0\n\ts_nop 0\n\tglobal_load_lds_dword %1, off sc1" :: "s"(L0 + 4u * PS), "v"(fp) : "memory");
    }
    DMA4(srdK, vkA, vkB, 3u * 16384u + wrow, L0 + 3u * PS + wrow, 0u);
    DMA4(srdV, vvA, vvA, 1u * 16384u + wrow, L0 + 3u * PS + VOFF + wrow, 1u);
    BAR_ONLY();
  }
#define QKBLOCK(C0, C1) do { \
    _Pragma("unroll") for (int m = 0; m < LA; ++m) fr[m] = KFR(m, sl); \
    SBAR(); \
    _Pragma("unroll") for (int m = 0; m < 16; ++m) { \
      if (m + LA < 16) fr[(m + LA) & 7] = KFR(m + LA, sl); \
      else fr[(m + LA) & 7] = VFR(m + LA - 16, sl); \
      if (m < 4) { sa[m][0] = MF16(fr[m], qf[0][0], C0); sa[m][1] = MF16(fr[m], qf[1][0], C1); } \
      else { sa[m & 3][0] = MF16(fr[m & 7], qf[0][m >> 2], sa[m & 3][0]); sa[m & 3][1] = MF16(fr[m & 7], qf[1][m >> 2], sa[m & 3][1]); } \
      SBAR(); } } while (0)
#define EXPS() do { ts0 = 0.f; ts1 = 0.f; \
      _Pragma("unroll") for (int kb = 0; kb < 4; ++kb) { _Pragma("unroll") for (int e = 0; e < 4; e += 2) { \
        const float a0 = __builtin_amdgcn_exp2f(sa[kb][0][e]), a1 = __builtin_amdgcn_exp2f(sa[kb][0][e + 1]), c0_ = __builtin_amdgcn_exp2f(sa[kb][1][e]), c1_ = __builtin_amdgcn_exp2f(sa[kb][1][e + 1]); \
        ts0 += a0; ts1 = __builtin_fmaf(c0_, one, ts1); ts0 += a1; ts1 = __builtin_fmaf(c1_, one, ts1); \
        pf[0][kb >> 1][2 * (kb & 1) + (e >> 1)] = cvtpk(a0, a1); pf[1][kb >> 1][2 * (kb & 1) + (e >> 1)] = cvtpk(c0_, c1_); } } } while (0)
#define ROWMAX(mx, qh) do { mx = sa[0][qh][0]; \
      _Pragma("unroll") for (int e = 1; e < 16; ++e) mx = __builtin_fmaxf(mx, sa[e >> 2][qh][e & 3]); \
      mx = grp_max(mx); } while (0)
#define SHIFT(dl, qh) do { _Pragma("unroll") for (int kb = 0; kb < 4; ++kb) { \
      sa[kb][qh][0] -= dl; sa[kb][qh][1] = __builtin_fmaf(sa[kb][qh][1], one, -dl); sa[kb][qh][2] -= dl; sa[kb][qh][3] = __builtin_fmaf(sa[kb][qh][3], one, -dl); } } while (0)
#define SETNEG() do { negm[0] = (f32x4){-mref0, -mref0, -mref0, -mref0}; negm[1] = (f32x4){-mref1, -mref1, -mref1, -mref1}; PIN(negm[0]); PIN(negm[1]); } while (0)
#define PINP() do { PIN(pf[0][0]); PIN(pf[0][1]); PIN(pf[1][0]); PIN(pf[1][1]); PIN(lsum0); PIN(lsum1); SBAR(); } while (0)
  float ts0, ts1;
  {
    const f32x4 zero = {};
    QKBLOCK(zero, zero);
    __builtin_amdgcn_s_setprio(PRIO_S);
    if (g) { WAIT_BAR(8); } else { BAR_ONLY(); }
    DMA4(srdK, vkA, vkB, (g + 4u) * 16384u + wrow, L0 + sl + wrow, 0u);
    ROWMAX(mref0, 0); ROWMAX(mref1, 1);
    SHIFT(mref0, 0); SHIFT(mref1, 1);
    SETNEG();
    EXPS();
    lsum0 = ts0; lsum1 = ts1;
    PINP();
    DMA4(srdV, vvA, vvA, (g + 2u) * 16384u + wrow, L0 + sl + VOFF + wrow, g);
    sl ^= 2u * PS;
    WAIT_BAR(8);
    STAMP(6);
  }
  for (unsigned t = 1; t < 16; ++t) {
    const unsigned i = 2u * t + g;
    __builtin_amdgcn_s_setprio(PRIO_M);
    QKBLOCK(negm[0], negm[1]);
    PVBLOCK();
    __builtin_amdgcn_s_setprio(PRIO_S);
    BAR_ONLY();
    const unsigned pn = i + 4u;
    if (pn <= 31u) DMA4(srdK, vkA, vkB, pn * 16384u + wrow, L0 + sl + wrow, 0u);
    else if (pn == 32u) DMA4(srdV, vvA, vvA, 30u * 16384u + wrow, L0 + sl + VOFF + wrow, 0u);
    EXPS();
    if (__builtin_expect(__any(!(ts0 + ts1 <= SUM_THR)), 0)) {
      float mx0, mx1; ROWMAX(mx0, 0); ROWMAX(mx1, 1);
      const float dl0 = __builtin_fmaxf(mx0, 0.f), dl1 = __builtin_fmaxf(mx1, 0.f);
      const float f0 = __builtin_amdgcn_exp2f(-dl0), f1 = __builtin_amdgcn_exp2f(-dl1);
      mref0 += dl0; mref1 += dl1; lsum0 *= f0; lsum1 *= f1;
      SHIFT(dl0, 0); SHIFT(dl1, 1);
#pragma unroll
      for (int d = 0; d < 8; ++d) { oa[d][0] *= f0; oa[d][1] *= f1; }
      SETNEG();
      EXPS();
    }
    lsum0 += ts0; lsum1 = __builtin_fmaf(ts1, one, lsum1);
    PINP();
    if (pn <= 31u) DMA4(srdV, vvA, vvA, (pn - 2u) * 16384u + wrow, L0 + sl + VOFF + wrow, g);
    else if (pn == 32u) DMA4(srdV, vvA, vvA, 31u * 16384u + wrow, L0 + sl + wrow, 1u);
    else if (pn == 33u) { WDMA4(0u, L0 + PS); WDMA4(1u, L0 + PS + VOFF); }
    else if (pn == 34u) { WDMA4(2u, L0 + 4u * PS); WDMA4(3u, L0 + 4u * PS + VOFF); }
    sl ^= 2u * PS;
    WAIT_BAR(8);
  }
  __builtin_amdgcn_s_setprio(0);
  STAMP(10); STAMPC(21);
  u32x4 wreg[8];
  u32x4 own[4], b0[4], b1[4], b2[4];
  const unsigned xr = (2u + g) * PS + 8192u * wq4;
  {
    const unsigned xw = L0 + (3u - g) * PS + 8192u * wq4 + vl16;
    unsigned vfE = vrdE - g * VOFF, vfO = vrdO - g * VOFF; PIN(vfE); PIN(vfO);
#define FDB(m) (((m) & 3) + ((m) < 8 ? 4 : 0))
#define VFL(m) VFX(vfE, vfO, FDB(m), ((m) >> 2) & 1)
#pragma unroll
    for (int m = 0; m < LA; ++m) fr[m] = VFL(m);
    SBAR();
    const unsigned swo = ((8u * hg + ((im + 4u + (w >> 1)) & 7u)) * 128u + 64u * (w & 1u)) * 1024u;
#pragma unroll
    for (int m = 0; m < 16; ++m) {
      if (m + LA < 16) fr[(m + LA) & 7] = VFL(m + LA);
      if (m < 8) wreg[m] = __builtin_amdgcn_raw_buffer_load_b128(srdO, vwo, swo + 8192u * m, WO_AUX);
      oa[FDB(m)][0] = MF16(fr[m & 7], BF8(pf[0][(m >> 2) & 1]), oa[FDB(m)][0]); oa[FDB(m)][1] = MF16(fr[m & 7], BF8(pf[1][(m >> 2) & 1]), oa[FDB(m)][1]);
      if (m >= 10 && m < 14) { lds_wrf4(xw + 2048u * (m - 10), oa[m - 6][0]); lds_wrf4(xw + 2048u * (m - 10) + 1024u, oa[m - 6][1]); }
      SBAR();
      if (m == 9 && !g) { BAR_ONLY(); }
    }
    lsum0 = grp_sum(lsum0); lsum1 = grp_sum(lsum1);
    lds_wrf4(L0 + VOFF + 1024u * w + vl16, (f32x4){mref0, lsum0, mref1, lsum1});
    if (g) { WAIT_BAR(8); } else { BAR_ONLY(); }
    STAMP(11);
    f32x4 oth[4][2];
#pragma unroll
    for (int q = 0; q < 8; ++q) oth[q >> 1][q & 1] = lds_rdf4(L0 + xr + vl16 + 1024u * q);
    const f32x4 st = lds_rdf4(L0 + VOFF + 1024u * (w ^ 4u) + vl16);
    float ca[2], cb[2];
#pragma unroll
    for (int qh = 0; qh < 2; ++qh) {
      const float mo = qh ? mref1 : mref0, lo = qh ? lsum1 : lsum0, mm = __builtin_fmaxf(mo, st[2 * qh]);
      const float fa = __builtin_amdgcn_exp2f(mo - mm), fb = __builtin_amdgcn_exp2f(st[2 * qh] - mm);
      const float inv = 1.0f / (fa * lo + fb * st[2 * qh + 1]);
      ca[qh] = fa * inv; cb[qh] = fb * inv;
    }
#pragma unroll
    for (int k = 0; k < 4; ++k) {
      const int ks = k >> 1, qh = k & 1;
      const f32x4 u0 = oa[2 * ks][qh] * ca[qh] + oth[2 * ks][qh] * cb[qh], u1 = oa[2 * ks + 1][qh] * ca[qh] + oth[2 * ks + 1][qh] * cb[qh];
      own[k] = (u32x4){cvtpk(u0[0], u0[1]), cvtpk(u0[2], u0[3]), cvtpk(u1[0], u1[1]), cvtpk(u1[2], u1[3])};
      __builtin_amdgcn_raw_buffer_store_b128(own[k], srdF, vl16 + 1024u * k, im * 32768u + sfo, 16);
    }
  }
  SBAR();
  f32x4 cc[4][2] = {};
  const unsigned ard = L0 + 1024u * (g4 >> 1) + trl + 8192u * g + 512u * g;
  bf16x8 aa[8], ab[8];
  unsigned rdy = 0u;
  const GAS u64* hpoll = flag1 + 16u * im + ((im + lane) & 7u);
#define UB(n) ((n) < 2 ? PS + (n) * VOFF : (n) < 4 ? 4u * PS + ((n) - 2u) * VOFF : (n) * VOFF)
#define ABASE(n) unsigned u_[4] = {ard + UB(n), (ard ^ 32u) + UB(n), (ard ^ 512u) + UB(n), (ard ^ 544u) + UB(n)}; PIN(u_[0]); PIN(u_[1]); PIN(u_[2]); PIN(u_[3])
#define ARD1(A, f) do { A[f] = cat8(lds_rdtr(u_[(f) & 3] + 4096u * ((f) >> 2)), lds_rdtr(u_[(f) & 3] + 4096u * ((f) >> 2) + 2048u)); } while (0)
#define OLOAD(B, n) do { const unsigned so_ = ((im + (n)) & 7u) * 32768u + sfo; \
    _Pragma("unroll") for (int k_ = 0; k_ < 4; ++k_) B[k_] = __builtin_amdgcn_raw_buffer_load_b128(srdF, vl16 + 1024u * k_, so_, 16); SBAR(); } while (0)
#define PROC(A, B, AN, nn) do { ABASE(nn); \
    _Pragma("unroll") for (int f = 0; f < 8; ++f) { \
      cc[f & 3][0] = MF16(A[f], BF8(B[2 * (f >> 2)]), cc[f & 3][0]); cc[f & 3][1] = MF16(A[f], BF8(B[2 * (f >> 2) + 1]), cc[f & 3][1]); SBAR(); \
      ARD1(AN, f); SBAR(); } } while (0)
#define PROCL(A, B) do { _Pragma("unroll") for (int f = 0; f < 8; ++f) { \
      cc[f & 3][0] = MF16(A[f], BF8(B[2 * (f >> 2)]), cc[f & 3][0]); cc[f & 3][1] = MF16(A[f], BF8(B[2 * (f >> 2) + 1]), cc[f & 3][1]); } SBAR(); } while (0)
#define NEED(n) do { unsigned spin_ = 0; \
    while (!((rdy >> (n)) & 1u) && ++spin_ < SPIN_CAP) { \
      rdy = (unsigned)__ballot(__hip_atomic_load(hpoll, __ATOMIC_RELAXED, __HIP_MEMORY_SCOPE_AGENT) == tag); \
      if ((rdy >> (n)) & 1u) break; __builtin_amdgcn_s_sleep(2); } \
    asm volatile("" ::: "memory"); } while (0)
  { ABASE(0); _Pragma("unroll") for (int f = 0; f < 8; ++f) ARD1(aa, f); SBAR(); }
  PROC(aa, own, ab, 1);
#pragma unroll
  for (int q = 0; q < 8; ++q) lds_wr128(L0 + xr + vl16 + 1024u * q, wreg[q]);
  WAIT_BAR(0);
  STAMP(12);
  if (tid < 8u) __hip_atomic_store(flag1 + 16u * tid + im, tag, __ATOMIC_RELAXED, __HIP_MEMORY_SCOPE_AGENT);
  NEED(1); OLOAD(b0, 1);
  STAMP(13);
  NEED(2); OLOAD(b1, 2);
  NEED(3); OLOAD(b2, 3);
  PROC(ab, b0, aa, 2);
  NEED(4); OLOAD(b0, 4);
  PROC(aa, b1, ab, 3);
  NEED(5); OLOAD(b1, 5);
  PROC(ab, b2, aa, 4);
  NEED(6); OLOAD(b2, 6);
  PROC(aa, b0, ab, 5);
  NEED(7); OLOAD(b0, 7);
  PROC(ab, b1, aa, 6);
  PROC(aa, b2, ab, 7);
  PROCL(ab, b0);
  STAMP(14);
  {
    const unsigned ra = L0 + g * VOFF + 4096u * wq4 + vl16, rb = L0 + (g ^ 1u) * VOFF + 4096u * wq4 + vl16;
#pragma unroll
    for (int q = 0; q < 4; ++q) lds_wrf4(ra + 1024u * q, cc[2 + (q >> 1)][q & 1]);
    BAR_ONLY();
    f32x4 kp[4];
#pragma unroll
    for (int q = 0; q < 4; ++q) kp[q] = cc[q >> 1][q & 1] + lds_rdf4(rb + 1024u * q);
    if (wq4 != hg) {
#pragma unroll
      for (int q = 0; q < 4; ++q) __builtin_amdgcn_raw_buffer_store_b128(__builtin_bit_cast(u32x4, kp[q]), srdP, vl16 + 1024u * q, spo, 16);
    }
    WAIT_BAR(0);
    STAMP(15);
    if (w == ((hg + 1u) & 3u) && lane < 4u) __hip_atomic_store(flag2 + 16u * lane + hg, tag, __ATOMIC_RELAXED, __HIP_MEMORY_SCOPE_AGENT);
    if (wq4 == hg) {
      poll_flags(flag2 + 16u * hg + (lane & 3u), tag, (lane & 3u) == hg);
      STAMP(16);
      u32x4 p0[4], p1[4], p2[4];
#pragma unroll
      for (int q = 0; q < 4; ++q) {
        p0[q] = __builtin_amdgcn_raw_buffer_load_b128(srdP, vl16 + 1024u * q, sfo + ((hg + 1u) & 3u) * 1048576u, 16);
        p1[q] = __builtin_amdgcn_raw_buffer_load_b128(srdP, vl16 + 1024u * q, sfo + ((hg + 2u) & 3u) * 1048576u, 16);
        p2[q] = __builtin_amdgcn_raw_buffer_load_b128(srdP, vl16 + 1024u * q, sfo + ((hg + 3u) & 3u) * 1048576u, 16);
      }
      if (h == 0u && tid == 0u)
        __hip_atomic_store((GAS u64*)(ws + WS_EPOCH + 256u * b), epoch + 1u, __ATOMIC_RELAXED, __HIP_MEMORY_SCOPE_AGENT);
      char* dst = out + ((size_t)(b * SQ + 32u * hg + l16) * DM + 64u * im + 32u * g + 4u * g4) * 2u;
#pragma unroll
      for (int q = 0; q < 4; ++q) {
        const f32x4 a = kp[q] + __builtin_bit_cast(f32x4, p0[q]) + __builtin_bit_cast(f32x4, p1[q]) + __builtin_bit_cast(f32x4, p2[q]);
        *(uint2*)(dst + (q & 1) * (16 * DM * 2) + (q >> 1) * 32) = make_uint2(cvtpk(a[0], a[1]), cvtpk(a[2], a[3]));
      }
    }
  }
}

extern "C" void kernel_launch(void* const* d_in, const int* in_sizes, int n_in, void* d_out, int out_size, void* d_ws, size_t ws_size, hipStream_t stream) {
  mha_kernel<<<256, 512, 0, stream>>>((const char*)d_in[0], (const char*)d_in[1], (const char*)d_in[2], (const char*)d_in[3], (const char*)d_in[4], (char*)d_out, (char*)d_ws + WS_BASE);
}
```

```cpp
#include <hip/hip_runtime.h>
#include <hip/hip_bf16.h>

typedef __bf16 bf16x8 __attribute__((ext_vector_type(8)));
typedef __bf16 bf16x4 __attribute__((ext_vector_type(4)));
typedef __bf16 bf16x2 __attribute__((ext_vector_type(2)));
typedef float f32x16 __attribute__((ext_vector_type(16)));
typedef float f32x4 __attribute__((ext_vector_type(4)));
typedef float f32x2 __attribute__((ext_vector_type(2)));
typedef unsigned u32x4 __attribute__((ext_vector_type(4)));
typedef unsigned long long u64;
#define LAS __attribute__((address_space(3)))
#define GAS __attribute__((address_space(1)))
#define DI __device__ __forceinline__
#define SBAR() __builtin_amdgcn_sched_barrier(0)
#define PIN(x) asm volatile("" : "+v"(x))
#define MFMA(a, b, c) __builtin_amdgcn_mfma_f32_32x32x16_bf16((a), (b), (c), 0, 0, 0)
#define MF16(a, b, c) __builtin_amdgcn_mfma_f32_16x16x32_bf16((a), (b), (c), 0, 0, 0)
#define BF8(x) __builtin_bit_cast(bf16x8, (x))

constexpr int SQ = 128, DM = 512, NH = 32, SK = 2048;
constexpr unsigned PS = 32768u;
constexpr unsigned VOFF = 16384u;
constexpr float QSCALE = 0.08838834764831845f * 1.4426950408889634f;
constexpr float SUM_THR = 65536.0f;
constexpr unsigned WS_EPOCH = 0u, WS_FLAG1 = 4096u, WS_FLAG2 = 65536u, WS_OFRAG = 1u << 20, WS_PART = 16u << 20;
constexpr unsigned SPIN_CAP = 1u << 18;

DI unsigned rfl(unsigned x) { return __builtin_amdgcn_readfirstlane(x); }
DI unsigned cvtpk(float lo, float hi) { f32x2 v = {lo, hi}; bf16x2 b = __builtin_convertvector(v, bf16x2); return __builtin_bit_cast(unsigned, b); }
DI bf16x8 lds_rd128(unsigned addr) { return *(const LAS bf16x8*)(size_t)addr; }
DI f32x4 lds_rdf4(unsigned addr) { return *(const LAS f32x4*)(size_t)addr; }
DI bf16x4 lds_rdtr(unsigned addr) { return __builtin_amdgcn_ds_read_tr16_b64_v4bf16((LAS bf16x4*)(size_t)addr); }
DI bf16x8 cat8(bf16x4 a, bf16x4 b) { return __builtin_shufflevector(a, b, 0, 1, 2, 3, 4, 5, 6, 7); }
DI void lds_wr128(unsigned addr, u32x4 v) { *(LAS u32x4*)(size_t)addr = v; }
DI void lds_wrf4(unsigned addr, f32x4 v) { *(LAS f32x4*)(size_t)addr = v; }
DI float grp_max(float v) {
  auto r = __builtin_amdgcn_permlane32_swap(__float_as_uint(v), __float_as_uint(v), false, false); v = __builtin_fmaxf(__uint_as_float(r[0]), __uint_as_float(r[1]));
  auto t = __builtin_amdgcn_permlane16_swap(__float_as_uint(v), __float_as_uint(v), false, false); return __builtin_fmaxf(__uint_as_float(t[0]), __uint_as_float(t[1])); }
DI float grp_sum(float v) {
  auto r = __builtin_amdgcn_permlane32_swap(__float_as_uint(v), __float_as_uint(v), false, false); v = __uint_as_float(r[0]) + __uint_as_float(r[1]);
  auto t = __builtin_amdgcn_permlane16_swap(__float_as_uint(v), __float_as_uint(v), false, false); return __uint_as_float(t[0]) + __uint_as_float(t[1]); }
DI void poll_flags(const GAS u64* p, u64 tag, bool skip) {
  for (unsigned spin = 0; spin < SPIN_CAP; ++spin) {
    const u64 v = __hip_atomic_load(p, __ATOMIC_RELAXED, __HIP_MEMORY_SCOPE_AGENT);
    if (__all(skip || v == tag)) break;
    __builtin_amdgcn_s_sleep(2);
  }
  asm volatile("" ::: "memory");
}

#define DMA4(srd, voffA, voffB, soff, m0a, par) \
  asm volatile("s_mov_b32 m0, %0\n\ts_nop 0\n\t" \
               "buffer_load_dwordx4 %2, %4, %5 offen nt lds\n\t" \
               "buffer_load_dwordx4 %3, %4, %5 offen offset:2048 nt lds\n\t" \
               "s_mov_b32 m0, %1\n\ts_nop 0\n\t" \
               "buffer_load_dwordx4 %2, %4, %6 offen nt lds\n\t" \
               "buffer_load_dwordx4 %3, %4, %6 offen offset:2048 nt lds" \
               :: "s"(m0a), "s"((m0a) + 1024u), "v"(voffA), "v"(voffB), "s"(srd), "s"((soff) + 128u * (par)), "s"((soff) + 128u - 128u * (par)) : "memory")
#define DMA2(srd, voffA, voffB, soff, m0a) \
  asm volatile("s_mov_b32 m0, %0\n\ts_nop 0\n\t" \
               "buffer_load_dwordx4 %1, %3, %4 offen nt lds\n\t" \
               "buffer_load_dwordx4 %2, %3, %4 offen offset:2048 nt lds" \
               :: "s"(m0a), "v"(voffA), "v"(voffB), "s"(srd), "s"(soff) : "memory")
#define DMA1(pol, srd, voff, soff, m0a) \
  asm volatile("s_mov_b32 m0, %0\n\ts_nop 0\n\tbuffer_load_dwordx4 %1, %2, %3 offen" pol " lds" :: "s"(m0a), "v"(voff), "s"(srd), "s"(soff) : "memory")

#define BAR_ONLY() asm volatile("s_waitcnt lgkmcnt(0)\n\ts_barrier" ::: "memory")
#define WAIT_BAR(N) asm volatile("s_waitcnt vmcnt(" #N ") lgkmcnt(0)\n\ts_barrier" ::: "memory")

__global__ __launch_bounds__(512, 2) void mha_kernel(const char* __restrict__ x, const char* __restrict__ kc, const char* __restrict__ vc,
                                                     const char* __restrict__ wq, const char* __restrict__ wo, char* __restrict__ out, char* ws) {
  __shared__ __attribute__((aligned(1024))) char lds[163840];
  const unsigned tid = threadIdx.x, lane = tid & 63u, l32 = lane & 31u, hi = lane >> 5;
  const unsigned w = rfl(tid >> 6), wq4 = w & 3u, g = w >> 2;
  const unsigned blk = blockIdx.x, xcd = blk & 7u, j = blk >> 3;
  const unsigned b = 4u * (xcd & 1u) + (j >> 3), hg = xcd >> 1, im = j & 7u, h = 8u * hg + im;
  const unsigned bh = b * NH + h;
  const unsigned L0 = (unsigned)(size_t)lds;

  const __amdgpu_buffer_rsrc_t srdX = __builtin_amdgcn_make_buffer_rsrc((void*)(x + (size_t)b * (SQ * DM * 2)), (short)0, SQ * DM * 2, 0x00020000);
  const __amdgpu_buffer_rsrc_t srdW = __builtin_amdgcn_make_buffer_rsrc((void*)(wq + (size_t)h * 256), (short)0, (int)(DM * 8192u - h * 256u), 0x00020000);
  const __amdgpu_buffer_rsrc_t srdK = __builtin_amdgcn_make_buffer_rsrc((void*)(kc + (size_t)bh * (SK * 256)), (short)0, SK * 256, 0x00020000);
  const __amdgpu_buffer_rsrc_t srdV = __builtin_amdgcn_make_buffer_rsrc((void*)(vc + (size_t)bh * (SK * 256)), (short)0, SK * 256, 0x00020000);

  const unsigned sub = lane >> 5, r8 = (lane >> 2) & 7u, slot = lane & 3u;
  const unsigned vx0 = (16u * w + r8) * 1024u + 64u * sub + 16u * (slot ^ (r8 >> 2));
  const unsigned vx1 = (16u * w + 8u + r8) * 1024u + 64u * sub + 16u * (slot ^ (2u + (r8 >> 2)));
  const unsigned vw = (8u * w + r8) * 8192u + 64u * sub + 16u * slot;
  const unsigned crot = h + 2u * b;
#define XB(c) (L0 + ((c) % 5 == 0 ? 3u * PS : (c) % 5 == 1 ? VOFF : (c) % 5 == 2 ? 4u * PS : (c) % 5 == 3 ? 0u : 2u * PS))
#define WB(c) (L0 + ((c) % 5 == 0 ? 3u * PS + VOFF : (c) % 5 == 1 ? PS + VOFF : (c) % 5 == 2 ? 4u * PS + VOFF : (c) % 5 == 3 ? PS : 2u * PS + VOFF))
#define QL(c, k) do { const unsigned cc_ = ((c) + crot) & 7u; \
    if ((k) == 0) DMA1("", srdX, vx0, 128u * cc_, XB(c) + 2048u * w); \
    else if ((k) == 1) DMA1("", srdX, vx1, 128u * cc_, XB(c) + 2048u * w + 1024u); \
    else if ((k) == 2) DMA1("", srdW, vw, 524288u * cc_, WB(c) + 2048u * w); \
    else DMA1("", srdW, vw, 524288u * cc_ + 128u, WB(c) + 2048u * w + 1024u); } while (0)
#define QLOAD(c) do { QL(c, 0); QL(c, 1); QL(c, 2); QL(c, 3); } while (0)
  QLOAD(0);
  asm volatile("s_barrier" ::: "memory");
  QLOAD(1);
  u64 epoch;
  asm volatile("s_load_dwordx2 %0, %1, 0x0\n\ts_waitcnt lgkmcnt(0)" : "=s"(epoch) : "s"(ws + WS_EPOCH + 256u * b) : "memory");
  const u64 tag = epoch * 0x9E3779B97F4A7C15ull + 0xD1B54A32D192ED03ull;
  const unsigned vkA = r8 * 256u + 64u * sub + 16u * (slot ^ (2u * (r8 >> 2)));
  const unsigned vkB = r8 * 256u + 64u * sub + 16u * (slot ^ (3u ^ (2u * (r8 >> 2))));
  const unsigned vvA = vkA;
  const unsigned wrow = wq4 * 4096u;
#define DA() DMA4(srdK, vkA, vkB, (g ^ 1u) * 16384u + wrow, L0 + (g ^ 1u) * PS + wrow, 0u)
#define DB(hf) do { if (g) DMA2(srdK, vkA, vkB, 2u * 16384u + wrow + 128u * (hf), L0 + 2u * PS + wrow + 1024u * (hf)); else DMA2(srdV, vvA, vvA, wrow + 128u * (hf), L0 + 2u * PS + VOFF + wrow + 1024u * (hf)); } while (0)
  const __amdgpu_buffer_rsrc_t srdO = __builtin_amdgcn_make_buffer_rsrc((void*)(wo + 128u * im), (short)0, (int)(4096u * 1024u - 128u * im), 0x00020000);
  const unsigned vwo = r8 * 1024u + 64u * sub + 16u * (slot ^ (2u * (r8 >> 2)));
#define WDMA4(n, ldsa) do { const unsigned so_ = ((8u * hg + ((im + (n)) & 7u)) * 128u + 32u * wq4) * 1024u; \
    DMA1("", srdO, vwo, so_, (ldsa) + wrow); DMA1("", srdO, vwo, so_ + 8192u, (ldsa) + wrow + 1024u); \
    DMA1("", srdO, vwo, so_ + 16384u, (ldsa) + wrow + 2048u); DMA1("", srdO, vwo, so_ + 24576u, (ldsa) + wrow + 3072u); } while (0)

  bf16x8 qf[2][4];
  const unsigned l16 = lane & 15u, g4 = lane >> 4;
  {
    f32x16 qa0 = {}, qa1 = {};
    const unsigned xrd0 = 4096u * wq4 + 1024u * (l32 >> 3) + 64u * (l32 & 7u) + 16u * ((0u + hi) ^ ((l32 >> 2) & 3u));
    const unsigned xrd1 = 4096u * wq4 + 1024u * (l32 >> 3) + 64u * (l32 & 7u) + 16u * ((2u + hi) ^ ((l32 >> 2) & 3u));
    const unsigned p = lane & 3u, psw = ((p & 1u) << 1) | (p >> 1);
    const unsigned wrd = 2048u * hi + 64u * ((lane >> 2) & 3u) + 32u * ((lane >> 4) & 1u) + 8u * psw + 1024u * g;
    bf16x8 fx[2][4], fa[2][4], fb[2][4];
#define QREAD1(c, s) do { \
      fx[(c) & 1][s] = lds_rd128(XB(c) + ((s & 1) ? xrd1 : xrd0) + 512u * (s >> 1)); \
      fa[(c) & 1][s] = cat8(lds_rdtr(WB(c) + wrd + 4096u * s), lds_rdtr(WB(c) + wrd + 4096u * s + 256u)); \
      fb[(c) & 1][s] = cat8(lds_rdtr(WB(c) + wrd + 4096u * s + 512u), lds_rdtr(WB(c) + wrd + 4096u * s + 768u)); SBAR(); } while (0)
#define QMM(c, s) do { qa0 = MFMA(fa[(c) & 1][s], fx[(c) & 1][s], qa0); qa1 = MFMA(fb[(c) & 1][s], fx[(c) & 1][s], qa1); SBAR(); } while (0)
#define QSUB(c, s, LD) do { QMM((c) - 1, s); if (LD) { QL((c) + 3, s); SBAR(); } QREAD1(c, s); } while (0)
#define QSTEP(c, N, LD) do { WAIT_BAR(N); QSUB(c, 0, LD); QSUB(c, 1, LD); QSUB(c, 2, LD); QSUB(c, 3, LD); } while (0)
    WAIT_BAR(4); QREAD1(0, 0); QREAD1(0, 1); QREAD1(0, 2); QREAD1(0, 3); QLOAD(2); QLOAD(3);
    QSTEP(1, 8, 1);
    QSTEP(2, 8, 1);
    QSTEP(3, 8, 1);
    QSTEP(4, 8, 1); DA();
    QSTEP(5, 12, 0); DB(0);
    QSTEP(6, 10, 0);
    QSTEP(7, 6, 0); DB(1);
    QMM(7, 0); QMM(7, 1); QMM(7, 2); QMM(7, 3);
    u32x4 mine[4];
#pragma unroll
    for (int sp = 0; sp < 2; ++sp) {
#pragma unroll
      for (int e = 0; e < 4; ++e) {
        mine[sp][e] = cvtpk(qa0[8 * sp + 2 * e] * QSCALE, qa0[8 * sp + 2 * e + 1] * QSCALE);
        mine[2 + sp][e] = cvtpk(qa1[8 * sp + 2 * e] * QSCALE, qa1[8 * sp + 2 * e + 1] * QSCALE);
      }
    }
    const unsigned qx = L0 + VOFF + (wq4 >> 1) * PS + (wq4 & 1u) * 8192u;
#pragma unroll
    for (int i = 0; i < 4; ++i) lds_wr128(qx + 4096u * g + 1024u * i + 16u * lane, mine[i]);
    WAIT_BAR(4);
#pragma unroll
    for (int ds = 0; ds < 4; ++ds) { qf[0][ds] = lds_rd128(qx + 512u * g4 + 16u * l16 + 2048u * ds); qf[1][ds] = lds_rd128(qx + 512u * g4 + 16u * l16 + 2048u * ds + 256u); }
  }
  const unsigned bsw = ((b & 3u) | ((b >> 2) << 4));
  const __amdgpu_buffer_rsrc_t srdF = __builtin_amdgcn_make_buffer_rsrc((void*)(ws + WS_OFRAG + (size_t)(bsw + 4u * hg) * 262144u), (short)0, 262144, 0x00020000);
  const __amdgpu_buffer_rsrc_t srdP = __builtin_amdgcn_make_buffer_rsrc((void*)(ws + WS_PART + (size_t)bsw * 262144u + im * 32768u), (short)0, 4 << 20, 0x00020000);
  const unsigned vl16 = 16u * lane;
  const unsigned sfo = (2u * wq4 + g) * 4096u, spo = hg * 1048576u + sfo;
  GAS u64* flag1 = (GAS u64*)(ws + WS_FLAG1 + (b * 4u + hg) * 1024u);
  GAS u64* flag2 = (GAS u64*)(ws + WS_FLAG2 + (b * 8u + im) * 512u);
  const unsigned q2 = l16 >> 2, sw = ((q2 << 1) ^ (3u * (q2 >> 1))) & 3u;
  const unsigned krd = L0 + 2048u * (l16 >> 3) + 64u * (l16 & 7u) + 16u * (g4 ^ sw);
  const unsigned trl = 256u * (g4 & 1u) + 64u * q2 + 32u * (g4 & 1u) + 8u * (lane & 3u);
  const unsigned vrdE = L0 + VOFF + 2048u * (g4 >> 1) + trl, vrdO = vrdE ^ 32u;
  f32x4 oa[8][2] = {}, sa[4][2], negm[2];
  float mref0 = 0.f, mref1 = 0.f, lsum0 = 0.f, lsum1 = 0.f;
  float one = 1.0f; PIN(one);
  u32x4 pf[2][2];
  bf16x8 fr[8];
#define KFR(m, sl) lds_rd128(krd + (sl) + 4096u * ((m) & 3) + 512u * ((m) >> 2))
#define VFX(vE, vO, db, ks) cat8(lds_rdtr((((db) & 1) ? vO : vE) + 8192u * (ks) + 512u * ((db) >> 1)), lds_rdtr((((db) & 1) ? vO : vE) + 8192u * (ks) + 512u * ((db) >> 1) + 4096u))
#define VFR(m, sl) VFX(vrdE + (sl), vrdO + (sl), (m) & 7, (m) >> 3)
#define PVBLOCK() do { _Pragma("unroll") for (int m = 0; m < 16; ++m) { \
      if (m + LA < 16) fr[(m + LA) & 7] = VFR(m + LA, sl); \
      oa[m & 7][0] = MF16(fr[m & 7], BF8(pf[0][m >> 3]), oa[m & 7][0]); oa[m & 7][1] = MF16(fr[m & 7], BF8(pf[1][m >> 3]), oa[m & 7][1]); \
      SBAR(); } } while (0)
  constexpr int LA = 6;
  unsigned sl = g * PS;
  if (g) {
    if (w == 4u) {
      const char* fp = (lane & 1u) ? (const char*)(flag2 + 16u * hg) : (const char*)(flag1 + 16u * im);
      asm volatile("s_mov_b32 m0, %0\n\ts_nop 0\n\tglobal_load_lds_dword %1, off sc1" :: "s"(L0 + 4u * PS), "v"(fp) : "memory");
    }
    DMA4(srdK, vkA, vkB, 3u * 16384u + wrow, L0 + 3u * PS + wrow, 0u);
    DMA4(srdV, vvA, vvA, 1u * 16384u + wrow, L0 + 3u * PS + VOFF + wrow, 1u);
    BAR_ONLY();
  }
#define QKBLOCK(C0, C1) do { \
    _Pragma("unroll") for (int m = 0; m < LA; ++m) fr[m] = KFR(m, sl); \
    SBAR(); \
    _Pragma("unroll") for (int m = 0; m < 16; ++m) { \
      if (m + LA < 16) fr[(m + LA) & 7] = KFR(m + LA, sl); \
      else fr[(m + LA) & 7] = VFR(m + LA - 16, sl); \
      if (m < 4) { sa[m][0] = MF16(fr[m], qf[0][0], C0); sa[m][1] = MF16(fr[m], qf[1][0], C1); } \
      else { sa[m & 3][0] = MF16(fr[m & 7], qf[0][m >> 2], sa[m & 3][0]); sa[m & 3][1] = MF16(fr[m & 7], qf[1][m >> 2], sa[m & 3][1]); } \
      SBAR(); } } while (0)
#define EXPS() do { ts0 = 0.f; ts1 = 0.f; \
      _Pragma("unroll") for (int kb = 0; kb < 4; ++kb) { _Pragma("unroll") for (int e = 0; e < 4; e += 2) { \
        const float a0 = __builtin_amdgcn_exp2f(sa[kb][0][e]), a1 = __builtin_amdgcn_exp2f(sa[kb][0][e + 1]), c0_ = __builtin_amdgcn_exp2f(sa[kb][1][e]), c1_ = __builtin_amdgcn_exp2f(sa[kb][1][e + 1]); \
        ts0 += a0; ts1 = __builtin_fmaf(c0_, one, ts1); ts0 += a1; ts1 = __builtin_fmaf(c1_, one, ts1); \
        pf[0][kb >> 1][2 * (kb & 1) + (e >> 1)] = cvtpk(a0, a1); pf[1][kb >> 1][2 * (kb & 1) + (e >> 1)] = cvtpk(c0_, c1_); } } } while (0)
#define ROWMAX(mx, qh) do { mx = sa[0][qh][0]; \
      _Pragma("unroll") for (int e = 1; e < 16; ++e) mx = __builtin_fmaxf(mx, sa[e >> 2][qh][e & 3]); \
      mx = grp_max(mx); } while (0)
#define SHIFT(dl, qh) do { _Pragma("unroll") for (int kb = 0; kb < 4; ++kb) { \
      sa[kb][qh][0] -= dl; sa[kb][qh][1] = __builtin_fmaf(sa[kb][qh][1], one, -dl); sa[kb][qh][2] -= dl; sa[kb][qh][3] = __builtin_fmaf(sa[kb][qh][3], one, -dl); } } while (0)
#define SETNEG() do { negm[0] = (f32x4){-mref0, -mref0, -mref0, -mref0}; negm[1] = (f32x4){-mref1, -mref1, -mref1, -mref1}; PIN(negm[0]); PIN(negm[1]); } while (0)
#define PINP() do { PIN(pf[0][0]); PIN(pf[0][1]); PIN(pf[1][0]); PIN(pf[1][1]); PIN(lsum0); PIN(lsum1); SBAR(); } while (0)
  float ts0, ts1;
  {
    const f32x4 zero = {};
    QKBLOCK(zero, zero);
    __builtin_amdgcn_s_setprio(0);
    if (g) { WAIT_BAR(8); } else { BAR_ONLY(); }
    DMA4(srdK, vkA, vkB, (g + 4u) * 16384u + wrow, L0 + sl + wrow, 0u);
    ROWMAX(mref0, 0); ROWMAX(mref1, 1);
    SHIFT(mref0, 0); SHIFT(mref1, 1);
    SETNEG();
    EXPS();
    lsum0 = ts0; lsum1 = ts1;
    PINP();
    DMA4(srdV, vvA, vvA, (g + 2u) * 16384u + wrow, L0 + sl + VOFF + wrow, g);
    sl ^= 2u * PS;
    WAIT_BAR(8);
  }
  for (unsigned t = 1; t < 16; ++t) {
    const unsigned i = 2u * t + g;
    __builtin_amdgcn_s_setprio(1);
    QKBLOCK(negm[0], negm[1]);
    PVBLOCK();
    __builtin_amdgcn_s_setprio(0);
    BAR_ONLY();
    const unsigned pn = i + 4u;
    if (pn <= 31u) DMA4(srdK, vkA, vkB, pn * 16384u + wrow, L0 + sl + wrow, 0u);
    else if (pn == 32u) DMA4(srdV, vvA, vvA, 30u * 16384u + wrow, L0 + sl + VOFF + wrow, 0u);
    EXPS();
    if (__builtin_expect(__any(!(ts0 + ts1 <= SUM_THR)), 0)) {
      float mx0, mx1; ROWMAX(mx0, 0); ROWMAX(mx1, 1);
      const float dl0 = __builtin_fmaxf(mx0, 0.f), dl1 = __builtin_fmaxf(mx1, 0.f);
      const float f0 = __builtin_amdgcn_exp2f(-dl0), f1 = __builtin_amdgcn_exp2f(-dl1);
      mref0 += dl0; mref1 += dl1; lsum0 *= f0; lsum1 *= f1;
      SHIFT(dl0, 0); SHIFT(dl1, 1);
#pragma unroll
      for (int d = 0; d < 8; ++d) { oa[d][0] *= f0; oa[d][1] *= f1; }
      SETNEG();
      EXPS();
    }
    lsum0 += ts0; lsum1 = __builtin_fmaf(ts1, one, lsum1);
    PINP();
    if (pn <= 31u) DMA4(srdV, vvA, vvA, (pn - 2u) * 16384u + wrow, L0 + sl + VOFF + wrow, g);
    else if (pn == 32u) DMA4(srdV, vvA, vvA, 31u * 16384u + wrow, L0 + sl + wrow, 1u);
    else if (pn == 33u) { WDMA4(0u, L0 + PS); WDMA4(1u, L0 + PS + VOFF); }
    else if (pn == 34u) { WDMA4(2u, L0 + 4u * PS); WDMA4(3u, L0 + 4u * PS + VOFF); }
    sl ^= 2u * PS;
    WAIT_BAR(8);
  }
  __builtin_amdgcn_s_setprio(0);
  u32x4 wreg[8];
  u32x4 own[4], b0[4], b1[4], b2[4];
  const unsigned xr = (2u + g) * PS + 8192u * wq4;
  {
    const unsigned xw = L0 + (3u - g) * PS + 8192u * wq4 + vl16;
    unsigned vfE = vrdE - g * VOFF, vfO = vrdO - g * VOFF; PIN(vfE); PIN(vfO);
#define FDB(m) (((m) & 3) + ((m) < 8 ? 4 : 0))
#define VFL(m) VFX(vfE, vfO, FDB(m), ((m) >> 2) & 1)
#pragma unroll
    for (int m = 0; m < LA; ++m) fr[m] = VFL(m);
    SBAR();
    const unsigned swo = ((8u * hg + ((im + 4u + (w >> 1)) & 7u)) * 128u + 64u * (w & 1u)) * 1024u;
#pragma unroll
    for (int m = 0; m < 16; ++m) {
      if (m + LA < 16) fr[(m + LA) & 7] = VFL(m + LA);
      if (m < 8) wreg[m] = __builtin_amdgcn_raw_buffer_load_b128(srdO, vwo, swo + 8192u * m, 0);
      oa[FDB(m)][0] = MF16(fr[m & 7], BF8(pf[0][(m >> 2) & 1]), oa[FDB(m)][0]); oa[FDB(m)][1] = MF16(fr[m & 7], BF8(pf[1][(m >> 2) & 1]), oa[FDB(m)][1]);
      if (m >= 10 && m < 14) { lds_wrf4(xw + 2048u * (m - 10), oa[m - 6][0]); lds_wrf4(xw + 2048u * (m - 10) + 1024u, oa[m - 6][1]); }
      SBAR();
      if (m == 9 && !g) { BAR_ONLY(); }
    }
    lsum0 = grp_sum(lsum0); lsum1 = grp_sum(lsum1);
    lds_wrf4(L0 + VOFF + 1024u * w + vl16, (f32x4){mref0, lsum0, mref1, lsum1});
    if (g) { WAIT_BAR(8); } else { BAR_ONLY(); }
    f32x4 oth[4][2];
#pragma unroll
    for (int q = 0; q < 8; ++q) oth[q >> 1][q & 1] = lds_rdf4(L0 + xr + vl16 + 1024u * q);
    const f32x4 st = lds_rdf4(L0 + VOFF + 1024u * (w ^ 4u) + vl16);
    float ca[2], cb[2];
#pragma unroll
    for (int qh = 0; qh < 2; ++qh) {
      const float mo = qh ? mref1 : mref0, lo = qh ? lsum1 : lsum0, mm = __builtin_fmaxf(mo, st[2 * qh]);
      const float fa = __builtin_amdgcn_exp2f(mo - mm), fb = __builtin_amdgcn_exp2f(st[2 * qh] - mm);
      const float inv = 1.0f / (fa * lo + fb * st[2 * qh + 1]);
      ca[qh] = fa * inv; cb[qh] = fb * inv;
    }
#pragma unroll
    for (int k = 0; k < 4; ++k) {
      const int ks = k >> 1, qh = k & 1;
      const f32x4 u0 = oa[2 * ks][qh] * ca[qh] + oth[2 * ks][qh] * cb[qh], u1 = oa[2 * ks + 1][qh] * ca[qh] + oth[2 * ks + 1][qh] * cb[qh];
      own[k] = (u32x4){cvtpk(u0[0], u0[1]), cvtpk(u0[2], u0[3]), cvtpk(u1[0], u1[1]), cvtpk(u1[2], u1[3])};
      __builtin_amdgcn_raw_buffer_store_b128(own[k], srdF, vl16 + 1024u * k, im * 32768u + sfo, 16);
    }
  }
  SBAR();
  f32x4 cc[4][2] = {};
  const unsigned ard = L0 + 1024u * (g4 >> 1) + trl + 8192u * g + 512u * g;
  bf16x8 aa[8], ab[8];
  unsigned rdy = 0u;
  const GAS u64* hpoll = flag1 + 16u * im + ((im + lane) & 7u);
#define UB(n) ((n) < 2 ? PS + (n) * VOFF : (n) < 4 ? 4u * PS + ((n) - 2u) * VOFF : (n) * VOFF)
#define ABASE(n) unsigned u_[4] = {ard + UB(n), (ard ^ 32u) + UB(n), (ard ^ 512u) + UB(n), (ard ^ 544u) + UB(n)}; PIN(u_[0]); PIN(u_[1]); PIN(u_[2]); PIN(u_[3])
#define ARD1(A, f) do { A[f] = cat8(lds_rdtr(u_[(f) & 3] + 4096u * ((f) >> 2)), lds_rdtr(u_[(f) & 3] + 4096u * ((f) >> 2) + 2048u)); } while (0)
#define OLOAD(B, n) do { const unsigned so_ = ((im + (n)) & 7u) * 32768u + sfo; \
    _Pragma("unroll") for (int k_ = 0; k_ < 4; ++k_) B[k_] = __builtin_amdgcn_raw_buffer_load_b128(srdF, vl16 + 1024u * k_, so_, 16); SBAR(); } while (0)
#define PROC(A, B, AN, nn) do { ABASE(nn); \
    _Pragma("unroll") for (int f = 0; f < 8; ++f) { \
      cc[f & 3][0] = MF16(A[f], BF8(B[2 * (f >> 2)]), cc[f & 3][0]); cc[f & 3][1] = MF16(A[f], BF8(B[2 * (f >> 2) + 1]), cc[f & 3][1]); SBAR(); \
      ARD1(AN, f); SBAR(); } } while (0)
#define PROCL(A, B) do { _Pragma("unroll") for (int f = 0; f < 8; ++f) { \
      cc[f & 3][0] = MF16(A[f], BF8(B[2 * (f >> 2)]), cc[f & 3][0]); cc[f & 3][1] = MF16(A[f], BF8(B[2 * (f >> 2) + 1]), cc[f & 3][1]); } SBAR(); } while (0)
#define NEED(n) do { unsigned spin_ = 0; \
    while (!((rdy >> (n)) & 1u) && ++spin_ < SPIN_CAP) { \
      rdy = (unsigned)__ballot(__hip_atomic_load(hpoll, __ATOMIC_RELAXED, __HIP_MEMORY_SCOPE_AGENT) == tag); \
      if ((rdy >> (n)) & 1u) break; __builtin_amdgcn_s_sleep(2); } \
    asm volatile("" ::: "memory"); } while (0)
  { ABASE(0); _Pragma("unroll") for (int f = 0; f < 8; ++f) ARD1(aa, f); SBAR(); }
  PROC(aa, own, ab, 1);
#pragma unroll
  for (int q = 0; q < 8; ++q) lds_wr128(L0 + xr + vl16 + 1024u * q, wreg[q]);
  WAIT_BAR(0);
  if (tid < 8u) __hip_atomic_store(flag1 + 16u * tid + im, tag, __ATOMIC_RELAXED, __HIP_MEMORY_SCOPE_AGENT);
  NEED(1); OLOAD(b0, 1);
  NEED(2); OLOAD(b1, 2);
  NEED(3); OLOAD(b2, 3);
  PROC(ab, b0, aa, 2);
  NEED(4); OLOAD(b0, 4);
  PROC(aa, b1, ab, 3);
  NEED(5); OLOAD(b1, 5);
  PROC(ab, b2, aa, 4);
  NEED(6); OLOAD(b2, 6);
  PROC(aa, b0, ab, 5);
  NEED(7); OLOAD(b0, 7);
  PROC(ab, b1, aa, 6);
  PROC(aa, b2, ab, 7);
  PROCL(ab, b0);
  {
    const unsigned ra = L0 + g * VOFF + 4096u * wq4 + vl16, rb = L0 + (g ^ 1u) * VOFF + 4096u * wq4 + vl16;
#pragma unroll
    for (int q = 0; q < 4; ++q) lds_wrf4(ra + 1024u * q, cc[2 + (q >> 1)][q & 1]);
    BAR_ONLY();
    f32x4 kp[4];
#pragma unroll
    for (int q = 0; q < 4; ++q) kp[q] = cc[q >> 1][q & 1] + lds_rdf4(rb + 1024u * q);
    if (wq4 != hg) {
#pragma unroll
      for (int q = 0; q < 4; ++q) __builtin_amdgcn_raw_buffer_store_b128(__builtin_bit_cast(u32x4, kp[q]), srdP, vl16 + 1024u * q, spo, 16);
    }
    WAIT_BAR(0);
    if (w == ((hg + 1u) & 3u) && lane < 4u) __hip_atomic_store(flag2 + 16u * lane + hg, tag, __ATOMIC_RELAXED, __HIP_MEMORY_SCOPE_AGENT);
    if (wq4 == hg) {
      poll_flags(flag2 + 16u * hg + (lane & 3u), tag, (lane & 3u) == hg);
      u32x4 p0[4], p1[4], p2[4];
#pragma unroll
      for (int q = 0; q < 4; ++q) {
        p0[q] = __builtin_amdgcn_raw_buffer_load_b128(srdP, vl16 + 1024u * q, sfo + ((hg + 1u) & 3u) * 1048576u, 16);
        p1[q] = __builtin_amdgcn_raw_buffer_load_b128(srdP, vl16 + 1024u * q, sfo + ((hg + 2u) & 3u) * 1048576u, 16);
        p2[q] = __builtin_amdgcn_raw_buffer_load_b128(srdP, vl16 + 1024u * q, sfo + ((hg + 3u) & 3u) * 1048576u, 16);
      }
      if (h == 0u && tid == 0u)
        __hip_atomic_store((GAS u64*)(ws + WS_EPOCH + 256u * b), epoch + 1u, __ATOMIC_RELAXED, __HIP_MEMORY_SCOPE_AGENT);
      char* dst = out + ((size_t)(b * SQ + 32u * hg + l16) * DM + 64u * im + 32u * g + 4u * g4) * 2u;
#pragma unroll
      for (int q = 0; q < 4; ++q) {
        const f32x4 a = kp[q] + __builtin_bit_cast(f32x4, p0[q]) + __builtin_bit_cast(f32x4, p1[q]) + __builtin_bit_cast(f32x4, p2[q]);
        *(uint2*)(dst + (q & 1) * (16 * DM * 2) + (q >> 1) * 32) = make_uint2(cvtpk(a[0], a[1]), cvtpk(a[2], a[3]));
      }
    }
  }
}

extern "C" void kernel_launch(void* const* d_in, const int* in_sizes, int n_in, void* d_out, int out_size, void* d_ws, size_t ws_size, hipStream_t stream) {
  mha_kernel<<<256, 512, 0, stream>>>((const char*)d_in[0], (const char*)d_in[1], (const char*)d_in[2], (const char*)d_in[3], (const char*)d_in[4], (char*)d_out, (char*)d_ws);
}
```
